```python
import torch

def _softmax(x, axis=-1):
    return torch.softmax(x, dim=axis)
def _gelu(x):
    return torch.nn.functional.gelu(x, approximate="tanh")
def _silu(x):
    return torch.nn.functional.silu(x)
def _sigmoid(x):
    return torch.sigmoid(x)
def _layernorm(x, axis=-1, eps=1e-5):
    m = x.mean(dim=axis, keepdim=True)
    v = x.var(dim=axis, keepdim=True, unbiased=False)
    return (x - m) / torch.sqrt(v + eps)
def _scatter_add(tgt, idx, src, axis=0):
    return tgt.clone().index_add_(axis, idx, src)
def _segment_sum(src, seg, n):
    out = torch.zeros((n,) + src.shape[1:], dtype=src.dtype)
    return out.index_add_(0, seg, src)
def _pad(x, pw):
    flat = tuple(v for pair in reversed(pw) for v in pair)
    return torch.nn.functional.pad(x, flat)

kernel_name = 'gated_mix__sliding_block_cached__dense_activation_squared_relu__decode'

def setup_inputs():

    _w = (lambda a: (lambda t: t.to(torch.bfloat16) if t.is_floating_point() else t)(torch.as_tensor(a)))
    def _A_setup():
        B, Sq, D = 2, 1, 576
        Hq, Hkv, Sk, Dh = 32, 16, 32768, 128
        assert Hq % Hkv == 0, (Hq, Hkv)
        return {
            "x": _w((lambda *s: torch.randn(*s, dtype=torch.bfloat16) * 0.05)(B, Sq, D)),
            "k_cache": _w((lambda *s: torch.randn(*s, dtype=torch.bfloat16) * 6.4)(B, Hkv, Sk, Dh)),
            "v_cache": _w((lambda *s: torch.randn(*s, dtype=torch.bfloat16) * 0.4)(B, Hkv, Sk, Dh)),
            "wq": _w((lambda *s: torch.randn(*s, dtype=torch.bfloat16) * 6.4)(D, Hq * Dh) * (D ** -0.5)),
            "wo": _w((lambda *s: torch.randn(*s, dtype=torch.bfloat16) * 0.4)(Hq * Dh, D) * ((Hq * Dh) ** -0.5)),
        }
    _A_in = _A_setup()

    def _B_setup():
        B, S, D, F = 2, 1, 576, 4608
        return {
            "x": _w((lambda *s: torch.randn(*s, dtype=torch.bfloat16) * 0.05)(B, S, D)),
            "w1": _w((lambda *s: torch.randn(*s, dtype=torch.bfloat16) * 0.05)(D, F)),
            "b1": _w((lambda *s: torch.randn(*s, dtype=torch.bfloat16) * 0.05)(F)),
            "w2": _w((lambda *s: torch.randn(*s, dtype=torch.bfloat16) * 0.05)(F, D)),
            "b2": _w((lambda *s: torch.randn(*s, dtype=torch.bfloat16) * 0.05)(D)),
        }
    _B_in = _B_setup()

    return {
        "A_x": _A_in["x"],
        "A_k_cache": _A_in["k_cache"],
        "A_v_cache": _A_in["v_cache"],
        "A_wq": _A_in["wq"],
        "A_wo": _A_in["wo"],
        "B_x": _B_in["x"],
        "B_w1": _B_in["w1"],
        "B_b1": _B_in["b1"],
        "B_w2": _B_in["w2"],
        "B_b2": _B_in["b2"],
        "gate_w": _w((lambda *s: torch.randn(*s, dtype=torch.bfloat16) * 0.05)(576) * 16.0),
    }

def reference(A_x, A_k_cache, A_v_cache, A_wq, A_wo, B_x, B_w1, B_b1, B_w2, B_b2, gate_w):
    def _A_ref(x, k_cache, v_cache, wq, wo):
        B, Sq, D = x.shape
        Hq, Dh = 32, 128
        Hkv, Sk = k_cache.shape[1], k_cache.shape[2]
        W = min(4096, max(256, Sk // 4))
        rep = Hq // Hkv
        q = (lambda x, *d: x.permute(*d))((x @ wq).reshape(B, Sq, Hq, Dh), 0, 2, 1, 3)
        k = (lambda x, n, axis: torch.repeat_interleave(x, n, dim=axis))(k_cache[:, :, Sk - W:, :], rep, axis=1)
        v = (lambda x, n, axis: torch.repeat_interleave(x, n, dim=axis))(v_cache[:, :, Sk - W:, :], rep, axis=1)
        scores = torch.einsum("bhqd,bhkd->bhqk", q, k) * (Dh ** -0.5)
        p = _softmax(scores, axis=-1)
        o = torch.einsum("bhqk,bhkd->bhqd", p, v)
        return (lambda x, *d: x.permute(*d))(o, 0, 2, 1, 3).reshape(B, Sq, Hq * Dh) @ wo

    def _B_ref(x, w1, b1, w2, b2):
        h = torch.einsum("bsd,df->bsf", x, w1) + b1
        zero = torch.zeros_like(h)
        r = torch.where(h > zero, h, zero)
        h = r * r
        out = torch.einsum("bsf,fd->bsd", h, w2) + b2
        return out

    x = A_x
    g = _sigmoid((x * gate_w).sum(-1, keepdims=True))
    a = _A_ref(x, A_k_cache, A_v_cache, A_wq, A_wo)
    b = _B_ref(x, B_w1, B_b1, B_w2, B_b2)
    return g * a + (1 - g) * b
```

```cpp
#include <hip/hip_runtime.h>
#include <hip/hip_bf16.h>
#include <stdint.h>

typedef unsigned short u16;
typedef unsigned int u32;
typedef __attribute__((ext_vector_type(4))) unsigned u32x4;

constexpr int B = 2, D = 576, HQ = 32, HKV = 16, SK = 32768, DH = 128, W = 4096, F = 4608;
constexpr int NSPLIT = 8, KPS = W / NSPLIT;

__device__ __forceinline__ float bf2f(u16 v) { return __uint_as_float(((u32)v) << 16); }
__device__ __forceinline__ float rbf(float f) {
  u32 u = __float_as_uint(f);
  u += 0x7FFFu + ((u >> 16) & 1u);
  return __uint_as_float(u & 0xFFFF0000u);
}
__device__ __forceinline__ u16 f2bf(float f) {
  u32 u = __float_as_uint(f);
  u += 0x7FFFu + ((u >> 16) & 1u);
  return (u16)(u >> 16);
}

__global__ void k_proj(const u16* x, const u16* wq, const u16* xb, const u16* w1, const u16* b1, const u16* xa, const u16* gw,
                       float* q, float* h, float* g) {
  __shared__ float xs[2][D];
  int n = blockIdx.x * blockDim.x + threadIdx.x;
  bool isq = blockIdx.x < 4096 / 256;
  const u16* xx = isq ? x : xb;
  for (int i = threadIdx.x; i < 2 * D; i += blockDim.x) xs[i / D][i % D] = bf2f(xx[i]);
  __syncthreads();
  if (isq) {
    float a0 = 0, a1 = 0;
    for (int k = 0; k < D; ++k) { float w = bf2f(wq[k * 4096 + n]); a0 += xs[0][k] * w; a1 += xs[1][k] * w; }
    q[n] = rbf(a0); q[4096 + n] = rbf(a1);
    if (n < 2) {
      float s = 0;
      for (int k = 0; k < D; ++k) s += rbf(bf2f(xa[n * D + k]) * bf2f(gw[k]));
      s = rbf(s);
      g[n] = rbf(1.f / (1.f + __expf(-s)));
    }
  } else {
    int f = n - 4096;
    float a0 = 0, a1 = 0;
    for (int k = 0; k < D; ++k) { float w = bf2f(w1[k * F + f]); a0 += xs[0][k] * w; a1 += xs[1][k] * w; }
    float bb = bf2f(b1[f]);
    a0 = rbf(rbf(a0) + bb); a1 = rbf(rbf(a1) + bb);
    a0 = a0 > 0 ? a0 : 0; a1 = a1 > 0 ? a1 : 0;
    h[f] = rbf(a0 * a0); h[F + f] = rbf(a1 * a1);
  }
}

__global__ void k_attn(const u16* K, const u16* V, const float* q, float* pm, float* pl, float* po) {
  int wg = blockIdx.x; int s = wg % NSPLIT; int hkv = (wg / NSPLIT) % HKV; int b = wg / (NSPLIT * HKV);
  __shared__ float qs[2][DH];
  __shared__ float sc[2][KPS];
  __shared__ float red[2][8];
  int t = threadIdx.x;
  if (t < 256) qs[t / DH][t % DH] = q[b * 4096 + hkv * 256 + t];
  __syncthreads();
  const u16* kp = K + ((size_t)(b * HKV + hkv) * SK + (SK - W) + s * KPS + t) * DH;
  float a0 = 0, a1 = 0;
  for (int d = 0; d < DH; d += 8) {
    u32x4 v = *(const u32x4*)(kp + d);
    for (int j = 0; j < 4; ++j) {
      float lo = __uint_as_float(v[j] << 16), hi = __uint_as_float(v[j] & 0xffff0000u);
      a0 += lo * qs[0][d + 2 * j] + hi * qs[0][d + 2 * j + 1];
      a1 += lo * qs[1][d + 2 * j] + hi * qs[1][d + 2 * j + 1];
    }
  }
  const float scale = 0.08838834764831845f;
  a0 = rbf(rbf(a0) * scale); a1 = rbf(rbf(a1) * scale);
  float m0 = a0, m1 = a1;
  for (int o = 32; o; o >>= 1) { m0 = fmaxf(m0, __shfl_xor(m0, o)); m1 = fmaxf(m1, __shfl_xor(m1, o)); }
  if ((t & 63) == 0) { red[0][t >> 6] = m0; red[1][t >> 6] = m1; }
  __syncthreads();
  m0 = red[0][0]; m1 = red[1][0];
  for (int i = 1; i < 8; ++i) { m0 = fmaxf(m0, red[0][i]); m1 = fmaxf(m1, red[1][i]); }
  __syncthreads();
  float p0 = __expf(a0 - m0), p1 = __expf(a1 - m1);
  sc[0][t] = p0; sc[1][t] = p1;
  float l0 = p0, l1 = p1;
  for (int o = 32; o; o >>= 1) { l0 += __shfl_xor(l0, o); l1 += __shfl_xor(l1, o); }
  if ((t & 63) == 0) { red[0][t >> 6] = l0; red[1][t >> 6] = l1; }
  __syncthreads();
  l0 = 0; l1 = 0;
  for (int i = 0; i < 8; ++i) { l0 += red[0][i]; l1 += red[1][i]; }
  if (t < 256) {
    int hh = t / DH, d = t % DH;
    const u16* vp = V + ((size_t)(b * HKV + hkv) * SK + (SK - W) + s * KPS) * DH + d;
    float acc = 0;
    for (int k = 0; k < KPS; ++k) acc += sc[hh][k] * bf2f(vp[(size_t)k * DH]);
    int hq = hkv * 2 + hh;
    po[((b * HQ + hq) * NSPLIT + s) * DH + d] = acc;
    if (d == 0) { pm[(b * HQ + hq) * NSPLIT + s] = hh ? m1 : m0; pl[(b * HQ + hq) * NSPLIT + s] = hh ? l1 : l0; }
  }
}

__global__ void k_comb(const float* pm, const float* pl, const float* po, float* o) {
  int bh = blockIdx.x, d = threadIdx.x;
  float M = -INFINITY;
  for (int s = 0; s < NSPLIT; ++s) M = fmaxf(M, pm[bh * NSPLIT + s]);
  float L = 0, acc = 0;
  for (int s = 0; s < NSPLIT; ++s) { float w = __expf(pm[bh * NSPLIT + s] - M); L += w * pl[bh * NSPLIT + s]; acc += w * po[(bh * NSPLIT + s) * DH + d]; }
  o[bh * DH + d] = rbf(acc / L);
}

__global__ void k_out(const float* o, const float* h, const float* g, const u16* wo, const u16* w2, const u16* b2, u16* out) {
  __shared__ float ra[16][64], rb[16][64];
  int b = blockIdx.x / 9, n = (blockIdx.x % 9) * 64 + (threadIdx.x & 63), kg = threadIdx.x >> 6;
  float a = 0, c = 0;
  for (int k = kg; k < 4096; k += 16) a += o[b * 4096 + k] * bf2f(wo[k * D + n]);
  for (int k = kg; k < F; k += 16) c += h[b * F + k] * bf2f(w2[k * D + n]);
  ra[kg][threadIdx.x & 63] = a; rb[kg][threadIdx.x & 63] = c;
  __syncthreads();
  if (kg == 0) {
    a = 0; c = 0;
    for (int i = 0; i < 16; ++i) { a += ra[i][threadIdx.x]; c += rb[i][threadIdx.x]; }
    a = rbf(a); c = rbf(rbf(c) + bf2f(b2[n]));
    float gg = g[b];
    float r = rbf(gg * a) + rbf(rbf(1.f - gg) * c);
    out[b * D + n] = f2bf(r);
  }
}

extern "C" void kernel_launch(void* const* d_in, const int* in_sizes, int n_in,
                              void* d_out, int out_size, void* d_ws, size_t ws_size,
                              hipStream_t stream) {
  const u16* A_x = (const u16*)d_in[0];
  const u16* Kc = (const u16*)d_in[1];
  const u16* Vc = (const u16*)d_in[2];
  const u16* wq = (const u16*)d_in[3];
  const u16* wo = (const u16*)d_in[4];
  const u16* B_x = (const u16*)d_in[5];
  const u16* w1 = (const u16*)d_in[6];
  const u16* b1 = (const u16*)d_in[7];
  const u16* w2 = (const u16*)d_in[8];
  const u16* b2 = (const u16*)d_in[9];
  const u16* gw = (const u16*)d_in[10];
  float* ws = (float*)d_ws;
  float* q = ws; float* h = q + 8192; float* g = h + 2 * F; float* pm = g + 64; float* pl = pm + 512; float* po = pl + 512; float* o = po + 512 * 128;
  k_proj<<<(4096 + F) / 256, 256, 0, stream>>>(A_x, wq, A_x, w1, b1, A_x, gw, q, h, g);
  k_attn<<<256, 512, 0, stream>>>(Kc, Vc, q, pm, pl, po);
  k_comb<<<64, 128, 0, stream>>>(pm, pl, po, o);
  k_out<<<18, 1024, 0, stream>>>(o, h, g, wo, w2, b2, (u16*)d_out);
}
```

```cpp
#include <hip/hip_runtime.h>
#include <stdint.h>

typedef unsigned short u16;
typedef unsigned int u32;
typedef unsigned long long u64;
typedef __attribute__((ext_vector_type(4))) unsigned u32x4;
typedef __attribute__((ext_vector_type(2))) unsigned u32x2;
typedef __attribute__((ext_vector_type(4))) float f32x4;
typedef __attribute__((ext_vector_type(2))) float f32x2;
typedef __attribute__((ext_vector_type(4))) short s16x4;
typedef __attribute__((ext_vector_type(2))) __bf16 bf16x2;
#define GAS __attribute__((address_space(1)))
#define LAS __attribute__((address_space(3)))

constexpr int D = 576, HKV = 16, SK = 32768, DH = 128, WIN = 4096, F = 4608;
constexpr int NWG = 256, NTHR = 1024;

constexpr u32 WS_E = 0;
constexpr u32 WS_QP = 256;
constexpr u32 WS_HP = WS_QP + 16 * 2 * 4 * 256 * 8;
constexpr u32 WS_P1 = WS_HP + 3 * 2 * F * 8;
constexpr u32 WS_P2 = WS_P1 + 16 * 8 * 16 * 288;
constexpr u32 WS_END = WS_P2 + 2 * 2 * 36 * 128 * 64;
constexpr u32 WS_STAMP = 64u << 20;

constexpr u32 L_XA = 0, L_XB = 3072, L_WQ = 6144, L_W1 = L_WQ + 18432, L_WO = L_W1 + 24576, L_W2 = L_WO + 18432;
constexpr u32 L_MRG = L_W2 + 21504;
constexpr u32 L_MM = L_MRG + 16384;
constexpr u32 L_ML = L_MM + 128;
constexpr u32 L_QS = L_ML + 128;
constexpr u32 L_AO = L_QS + 512;
constexpr u32 L_AH = L_AO + 128;
constexpr u32 L_FLAG = L_AH + 192;
constexpr u32 L_FIN = L_FLAG + 64;
constexpr u32 L_END = L_FIN + 256;

struct Params {
  const u16* x; const u16* K; const u16* V; const u16* wq; const u16* wo; const u16* w1;
  const u16* b1; const u16* w2; const u16* b2; const u16* gw; u16* out; unsigned char* ws;
};

constexpr int AUX_NT = 2, AUX_SC1 = 16;
#define W_NT 1
constexpr u32 SPIN_CAP = 1u << 16;

__device__ __forceinline__ float bf2f(u32 v16) { return __uint_as_float(v16 << 16); }
__device__ __forceinline__ u32 cvtpk(float lo, float hi) {
  u32 r; asm("v_cvt_pk_bf16_f32 %0, %1, %2" : "=v"(r) : "v"(lo), "v"(hi)); return r;
}
__device__ __forceinline__ float rbf(float f) { return __uint_as_float(cvtpk(f, f) << 16); }
__device__ __forceinline__ float dot2(u32 a, u32 b, float acc) {
  return __builtin_amdgcn_fdot2_f32_bf16(__builtin_bit_cast(bf16x2, a), __builtin_bit_cast(bf16x2, b), acc, false);
}
template <int CTRL> __device__ __forceinline__ float dpp(float x) {
  return __builtin_bit_cast(float, __builtin_amdgcn_mov_dpp(__builtin_bit_cast(int, x), CTRL, 0xf, 0xf, true));
}
constexpr int XOR1 = 0xB1, XOR2 = 0x4E, XOR7 = 0x141, XOR8 = 0x128, ROR4 = 0x124, ROR8 = 0x128, BCAST = 0x150;
__device__ __forceinline__ float x16_max(float x) {
  auto s = __builtin_amdgcn_permlane16_swap(__float_as_uint(x), __float_as_uint(x), false, false);
  return fmaxf(__uint_as_float(s[0]), __uint_as_float(s[1]));
}
__device__ __forceinline__ float x32_max(float x) {
  auto s = __builtin_amdgcn_permlane32_swap(__float_as_uint(x), __float_as_uint(x), false, false);
  return fmaxf(__uint_as_float(s[0]), __uint_as_float(s[1]));
}
__device__ __forceinline__ float x16_sum(float x) {
  auto s = __builtin_amdgcn_permlane16_swap(__float_as_uint(x), __float_as_uint(x), false, false);
  return __uint_as_float(s[0]) + __uint_as_float(s[1]);
}
__device__ __forceinline__ float x32_sum(float x) {
  auto s = __builtin_amdgcn_permlane32_swap(__float_as_uint(x), __float_as_uint(x), false, false);
  return __uint_as_float(s[0]) + __uint_as_float(s[1]);
}
template <int NT> __device__ __forceinline__ void glds16(const void* sbase, u32 voff, u32 lds_dst) {
  unsigned keep;
  if (NT)
    asm volatile("s_mov_b32 %0, m0\n\ts_mov_b32 m0, %3\n\ts_nop 0\n\tglobal_load_lds_dwordx4 %1, %2 nt\n\ts_mov_b32 m0, %0" : "=&s"(keep) : "v"(voff), "s"(sbase), "s"(lds_dst) : "memory");
  else
    asm volatile("s_mov_b32 %0, m0\n\ts_mov_b32 m0, %3\n\ts_nop 0\n\tglobal_load_lds_dwordx4 %1, %2\n\ts_mov_b32 m0, %0" : "=&s"(keep) : "v"(voff), "s"(sbase), "s"(lds_dst) : "memory");
}
__device__ __forceinline__ f32x4 mfma16(s16x4 a, s16x4 b, f32x4 c) { return __builtin_amdgcn_mfma_f32_16x16x16bf16_1k(a, b, c, 0, 0, 0); }
__device__ __forceinline__ s16x4 lds_tr(LAS unsigned char* L, u32 off) { return __builtin_amdgcn_ds_read_tr16_b64_v4i16((LAS s16x4*)(L + off)); }
__device__ __forceinline__ s16x4 lds_b64(LAS unsigned char* L, u32 off) { return *(LAS s16x4*)(L + off); }

#define STAMP(i) do {} while (0)

__global__ void __launch_bounds__(NTHR, 4) fused_kernel(Params p) {
  extern __shared__ __attribute__((aligned(16))) unsigned char smem[];
  LAS unsigned char* L = (LAS unsigned char*)smem;
  const u32 lb = (u32)(uintptr_t)L;
  const int tid = threadIdx.x, lane = tid & 63, w = __builtin_amdgcn_readfirstlane(tid >> 6);
  const int wg = blockIdx.x;
  const int xcd = wg & 7, idx = wg >> 3, hkv = xcd * 2 + (idx >> 4), ii = idx & 15, b = ii >> 3, s = ii & 7;
  const int rb = ii >> 1, cg = ii & 1, u = hkv * 8 + rb;
  const __amdgpu_buffer_rsrc_t rws = __builtin_amdgcn_make_buffer_rsrc((void*)p.ws, 0, WS_END, 0x00020000);
  STAMP(0);
  const u32 Eraw = __hip_atomic_load((GAS u32*)(p.ws + WS_E), __ATOMIC_RELAXED, __HIP_MEMORY_SCOPE_AGENT);

  const int g4 = lane >> 4, col = lane & 15, q4 = col >> 2, p4 = lane & 3;
  if (w == 0) {
    const int cb = ii & 3, kq = ii >> 2;
    glds16<0>(p.x, lane * 16, lb + L_XA); glds16<0>(p.x, 1024 + lane * 16, lb + L_XA + 1024);
    if (lane < 16) glds16<0>(p.x, 2048 + lane * 16, lb + L_XA + 2048);
    const u32 voff = (lane >> 3) * 8192 + (lane & 7) * 16;
    const char* base = (const char*)p.wq + (size_t)(144 * kq) * 8192 + (hkv * 256 + 64 * cb) * 2;
#pragma unroll
    for (int j = 0; j < 18; ++j) glds16<W_NT>(base + (size_t)j * 8 * 8192, voff, lb + L_WQ + j * 1024);
  } else if (w == 1) {
    if (wg < 216) {
      const int cb = wg / 3, kt = wg - 3 * cb;
      glds16<0>(p.x, lane * 16, lb + L_XB); glds16<0>(p.x, 1024 + lane * 16, lb + L_XB + 1024);
      if (lane < 16) glds16<0>(p.x, 2048 + lane * 16, lb + L_XB + 2048);
      const u32 voff = (lane >> 3) * 9216 + (lane & 7) * 16;
      const char* base = (const char*)p.w1 + (size_t)(192 * kt) * 9216 + cb * 128;
#pragma unroll
      for (int j = 0; j < 24; ++j) glds16<W_NT>(base + (size_t)j * 8 * 9216, voff, lb + L_W1 + j * 1024);
    }
  } else if (w == 2) {
    const char* base = (const char*)p.wo + (size_t)(hkv * 256 + 32 * rb) * 1152 + cg * 576;
#pragma unroll
    for (int j = 0; j < 18; ++j) { const u32 c = 64 * j + lane; const u32 row = (c * 1821u) >> 16; glds16<W_NT>(base, row * 576 + c * 16, lb + L_WO + j * 1024); }
  } else if (w == 3) {
    const char* base = (const char*)p.w2 + (size_t)(36 * u) * 1152 + cg * 576;
#pragma unroll
    for (int j = 0; j < 21; ++j) { const u32 c = 64 * j + lane; const u32 row = (c * 1821u) >> 16; if (j < 20 || lane < 16) glds16<W_NT>(base, row * 576 + c * 16, lb + L_W2 + j * 1024); }
  }
  u32x4 gx0[2], gx1[2], ggw[2];
  if (w == 2) {
    const GAS u32x4* xp = (const GAS u32x4*)p.x; const GAS u32x4* gp = (const GAS u32x4*)p.gw;
    gx0[0] = xp[lane]; gx1[0] = xp[72 + lane]; ggw[0] = gp[lane];
    const int l2 = 64 + (lane & 7);
    gx0[1] = xp[l2]; gx1[1] = xp[72 + l2]; ggw[1] = gp[l2];
  }
  const __amdgpu_buffer_rsrc_t rk = __builtin_amdgcn_make_buffer_rsrc((void*)p.K, 0, 0x10000000, 0x00020000);
  const __amdgpu_buffer_rsrc_t rv = __builtin_amdgcn_make_buffer_rsrc((void*)p.V, 0, 0x10000000, 0x00020000);
  const u32 kvoff = (u32)(((u32)(b * HKV + hkv) * SK + (SK - WIN) + s * 512 + w * 32) * (DH * 2));
  u32x4 kk[8], vv[8];
#pragma unroll
  for (int j = 0; j < 8; ++j) kk[j] = __builtin_amdgcn_raw_buffer_load_b128(rk, lane * 16, kvoff + j * 1024, AUX_NT);
#pragma unroll
  for (int j = 0; j < 8; ++j) vv[j] = __builtin_amdgcn_raw_buffer_load_b128(rv, lane * 16, kvoff + j * 1024, AUX_NT);
  __builtin_amdgcn_sched_barrier(0);
  STAMP(1);
  const u32 tag = __builtin_amdgcn_readfirstlane(Eraw) + 1u;

  if (w == 0) {
    asm volatile("s_waitcnt vmcnt(16)" ::: "memory");
    STAMP(2);
    const int cb = ii & 3, kq = ii >> 2;
    f32x4 acc[4];
#pragma unroll
    for (int t = 0; t < 4; ++t) acc[t] = f32x4{0.f, 0.f, 0.f, 0.f};
    const u32 aoff = L_XA + (lane & 1) * 1152 + (144 * kq + 4 * g4) * 2;
    const u32 boff = L_WQ + (4 * g4 + q4) * 128 + p4 * 8;
#pragma unroll
    for (int st = 0; st < 9; ++st) {
      const s16x4 a = lds_b64(L, aoff + st * 32);
#pragma unroll
      for (int t = 0; t < 4; ++t) acc[t] = mfma16(a, lds_tr(L, boff + st * 2048 + t * 32), acc[t]);
    }
    const float v0 = g4 == 0 ? acc[0][0] : g4 == 1 ? acc[1][0] : g4 == 2 ? acc[2][0] : acc[3][0];
    const float v1 = g4 == 0 ? acc[0][1] : g4 == 1 ? acc[1][1] : g4 == 2 ? acc[2][1] : acc[3][1];
    const u32 o0 = WS_QP + (((hkv * 2 + 0) * 4 + kq) * 256 + 64 * cb + lane) * 8;
    __builtin_amdgcn_raw_buffer_store_b64(u32x2{__float_as_uint(v0), tag}, rws, o0, 0, AUX_SC1);
    __builtin_amdgcn_raw_buffer_store_b64(u32x2{__float_as_uint(v1), tag}, rws, o0 + 4 * 256 * 8, 0, AUX_SC1);
    STAMP(3);
    const u32 go = WS_QP + (((hkv * 2 + b) * 4) * 256 + 2 * lane) * 8;
    u32x4 qg[8];
    for (u32 spin = 0;; ++spin) {
#pragma unroll
      for (int j = 0; j < 8; ++j) qg[j] = __builtin_amdgcn_raw_buffer_load_b128(rws, go + (j >> 1) * 2048 + (j & 1) * 1024, 0, AUX_SC1);
      bool ok = true;
#pragma unroll
      for (int j = 0; j < 8; ++j) ok &= (qg[j][1] == tag) & (qg[j][3] == tag);
      if (__all(ok) || spin > SPIN_CAP) break;
      __builtin_amdgcn_s_sleep(2);
    }
#pragma unroll
    for (int h = 0; h < 2; ++h) {
      float a0 = 0.f, a1 = 0.f;
#pragma unroll
      for (int kq2 = 0; kq2 < 4; ++kq2) { a0 += __uint_as_float(qg[kq2 * 2 + h][0]); a1 += __uint_as_float(qg[kq2 * 2 + h][2]); }
      *(LAS u32*)(L + L_QS + h * 256 + lane * 4) = cvtpk(a0, a1);
    }
    asm volatile("s_waitcnt lgkmcnt(0)" ::: "memory");
    if (lane == 0) __hip_atomic_store((LAS u32*)(L + L_FLAG), tag, __ATOMIC_RELAXED, __HIP_MEMORY_SCOPE_WORKGROUP);
    STAMP(4);
  } else if (w == 1) {
    if (wg < 216) {
      asm volatile("s_waitcnt vmcnt(16)" ::: "memory");
      const int cb = wg / 3, kt = wg - 3 * cb;
      f32x4 acc[4];
#pragma unroll
      for (int t = 0; t < 4; ++t) acc[t] = f32x4{0.f, 0.f, 0.f, 0.f};
      const u32 aoff = L_XB + (lane & 1) * 1152 + (192 * kt + 4 * g4) * 2;
      const u32 boff = L_W1 + (4 * g4 + q4) * 128 + p4 * 8;
#pragma unroll
      for (int st = 0; st < 12; ++st) {
        const s16x4 a = lds_b64(L, aoff + st * 32);
#pragma unroll
        for (int t = 0; t < 4; ++t) acc[t] = mfma16(a, lds_tr(L, boff + st * 2048 + t * 32), acc[t]);
      }
      const float v0 = g4 == 0 ? acc[0][0] : g4 == 1 ? acc[1][0] : g4 == 2 ? acc[2][0] : acc[3][0];
      const float v1 = g4 == 0 ? acc[0][1] : g4 == 1 ? acc[1][1] : g4 == 2 ? acc[2][1] : acc[3][1];
      const u32 o0 = WS_HP + ((kt * 2 + 0) * F + 64 * cb + lane) * 8;
      __builtin_amdgcn_raw_buffer_store_b64(u32x2{__float_as_uint(v0), tag}, rws, o0, 0, AUX_SC1);
      __builtin_amdgcn_raw_buffer_store_b64(u32x2{__float_as_uint(v1), tag}, rws, o0 + F * 8, 0, AUX_SC1);
    }
  } else if (w == 2) {
    float s0 = 0.f, s1 = 0.f;
#pragma unroll
    for (int r = 0; r < 2; ++r) {
      float t0 = 0.f, t1 = 0.f;
#pragma unroll
      for (int j = 0; j < 4; ++j) {
        const float glo = bf2f(ggw[r][j] & 0xffffu), ghi = __uint_as_float(ggw[r][j] & 0xffff0000u);
        t0 += rbf(bf2f(gx0[r][j] & 0xffffu) * glo) + rbf(__uint_as_float(gx0[r][j] & 0xffff0000u) * ghi);
        t1 += rbf(bf2f(gx1[r][j] & 0xffffu) * glo) + rbf(__uint_as_float(gx1[r][j] & 0xffff0000u) * ghi);
      }
      if (r == 0 || lane < 8) { s0 += t0; s1 += t1; }
    }
    s0 += dpp<XOR1>(s0); s0 += dpp<XOR2>(s0); s0 += dpp<ROR4>(s0); s0 += dpp<ROR8>(s0); s0 = x32_sum(x16_sum(s0));
    s1 += dpp<XOR1>(s1); s1 += dpp<XOR2>(s1); s1 += dpp<ROR4>(s1); s1 += dpp<ROR8>(s1); s1 = x32_sum(x16_sum(s1));
    const float g0 = rbf(1.f / (1.f + __expf(-rbf(s0)))), g1 = rbf(1.f / (1.f + __expf(-rbf(s1))));
    if (lane == 0) { LAS float* fl = (LAS float*)(L + L_FLAG); fl[1] = g0; fl[2] = g1; fl[3] = rbf(1.f - g0); fl[4] = rbf(1.f - g1); }
  }

  for (u32 spin = 0; spin < (1u << 22); ++spin) {
    if (__hip_atomic_load((LAS u32*)(L + L_FLAG), __ATOMIC_RELAXED, __HIP_MEMORY_SCOPE_WORKGROUP) == tag) break;
    __builtin_amdgcn_s_sleep(1);
  }
  asm volatile("" ::: "memory");
  const u32x4 q0 = *(LAS u32x4*)(L + L_QS + col * 16), q1 = *(LAS u32x4*)(L + L_QS + 256 + col * 16);
  STAMP(5);
  float x[16];
#pragma unroll
  for (int i = 0; i < 8; ++i) {
    float d0 = 0.f, d1 = 0.f;
#pragma unroll
    for (int j = 0; j < 4; ++j) { d0 = dot2(kk[i][j], q0[j], d0); d1 = dot2(kk[i][j], q1[j], d1); }
    x[2 * i] = d0; x[2 * i + 1] = d1;
  }
#define TR_STEP(HALF, CTRL, BIT) _Pragma("unroll") for (int h = 0; h < (HALF); ++h) { \
    const float lo_ = x[h], hi_ = x[h + (HALF)]; const float t1_ = lo_ + dpp<CTRL>(lo_), t2_ = hi_ + dpp<CTRL>(hi_); \
    x[h] = (col & (BIT)) ? t2_ : t1_; }
  TR_STEP(8, XOR8, 8) TR_STEP(4, XOR7, 4) TR_STEP(2, XOR2, 2) TR_STEP(1, XOR1, 1)
#undef TR_STEP
  const float sc = rbf(rbf(x[0]) * 0.08838834764831845f);
  float mx = sc;
  mx = fmaxf(mx, dpp<XOR2>(mx)); mx = fmaxf(mx, dpp<ROR4>(mx)); mx = fmaxf(mx, dpp<ROR8>(mx)); mx = x32_max(x16_max(mx));
  const float pr = __builtin_amdgcn_exp2f((sc - mx) * 1.4426950408889634f);
  STAMP(6);
  f32x2 a0[4], a1[4];
#pragma unroll
  for (int j = 0; j < 4; ++j) { a0[j] = f32x2{0.f, 0.f}; a1[j] = f32x2{0.f, 0.f}; }
#define PV(i) { const float p0_ = dpp<BCAST + 2 * (i)>(pr), p1_ = dpp<BCAST + 2 * (i) + 1>(pr); \
    _Pragma("unroll") for (int j = 0; j < 4; ++j) { const f32x2 vf = {__uint_as_float(vv[i][j] << 16), __uint_as_float(vv[i][j] & 0xffff0000u)}; \
      a0[j] = __builtin_elementwise_fma(vf, f32x2{p0_, p0_}, a0[j]); a1[j] = __builtin_elementwise_fma(vf, f32x2{p1_, p1_}, a1[j]); } }
  PV(0) PV(1) PV(2) PV(3) PV(4) PV(5) PV(6) PV(7)
#undef PV
  float ls = pr;
  ls += dpp<XOR2>(ls); ls += dpp<ROR4>(ls); ls += dpp<ROR8>(ls); ls = x32_sum(x16_sum(ls));
  float y[8];
#pragma unroll
  for (int k = 0; k < 8; ++k) {
    auto r = __builtin_amdgcn_permlane32_swap(__float_as_uint(a0[k >> 1][k & 1]), __float_as_uint(a1[k >> 1][k & 1]), false, false);
    y[k] = __uint_as_float(r[0]) + __uint_as_float(r[1]);
  }
  f32x4 z;
#pragma unroll
  for (int k = 0; k < 4; ++k) {
    auto r = __builtin_amdgcn_permlane16_swap(__float_as_uint(y[k]), __float_as_uint(y[k + 4]), false, false);
    z[k] = __uint_as_float(r[0]) + __uint_as_float(r[1]);
  }
  *(LAS f32x4*)(L + L_MRG + w * 1024 + ((g4 >> 1) * 128 + 8 * col + 4 * (g4 & 1)) * 4) = z;
  if (lane < 2) { *(LAS float*)(L + L_MM + lane * 64 + w * 4) = mx; *(LAS float*)(L + L_ML + lane * 64 + w * 4) = ls; }
  STAMP(7);
  if (w == 1) {
    const int cc = lane < 36 ? lane : 35;
    const u32 ho = WS_HP + (36 * u + cc) * 8;
    u32x2 hg[6];
    const float bb = bf2f(p.b1[36 * u + cc]);
    for (u32 spin = 0;; ++spin) {
#pragma unroll
      for (int j = 0; j < 6; ++j) hg[j] = __builtin_amdgcn_raw_buffer_load_b64(rws, ho + j * F * 8, 0, AUX_SC1);
      bool ok = true;
#pragma unroll
      for (int j = 0; j < 6; ++j) ok &= hg[j][1] == tag;
      if (__all(ok) || spin > SPIN_CAP) break;
      __builtin_amdgcn_s_sleep(2);
    }
#pragma unroll
    for (int bb2 = 0; bb2 < 2; ++bb2) {
      float hs = __uint_as_float(hg[bb2][0]) + __uint_as_float(hg[2 + bb2][0]) + __uint_as_float(hg[4 + bb2][0]);
      hs = rbf(rbf(hs) + bb);
      hs = hs > 0.f ? hs : 0.f;
      const u32 hv = lane < 36 ? (cvtpk(hs * hs, 0.f) & 0xffffu) : 0u;
      if (lane < 48) *(LAS u16*)(L + L_AH + bb2 * 96 + lane * 2) = (u16)hv;
    }
  }
  __syncthreads();
  STAMP(8);
  if (w < 4) {
    const int h = tid >> 7, d = tid & 127;
    f32x4 mv[4], lv[4];
#pragma unroll
    for (int j = 0; j < 4; ++j) { mv[j] = *(LAS f32x4*)(L + L_MM + h * 64 + j * 16); lv[j] = *(LAS f32x4*)(L + L_ML + h * 64 + j * 16); }
    float M = mv[0][0];
#pragma unroll
    for (int j = 1; j < 16; ++j) M = fmaxf(M, mv[j >> 2][j & 3]);
    float o = 0.f, Ls = 0.f;
#pragma unroll
    for (int j = 0; j < 16; ++j) {
      const float e = __builtin_amdgcn_exp2f((mv[j >> 2][j & 3] - M) * 1.4426950408889634f);
      o = fmaf(e, *(LAS float*)(L + L_MRG + j * 1024 + tid * 4), o);
      Ls = fmaf(e, lv[j >> 2][j & 3], Ls);
    }
    const u32 rec = WS_P1 + (((hkv * 8 + h * 4 + (d >> 5)) * 16) + b * 8 + s) * 288;
    __builtin_amdgcn_raw_buffer_store_b64(u32x2{__float_as_uint(o), tag}, rws, rec + (d & 31) * 8, 0, AUX_SC1);
    if ((d & 31) < 2) __builtin_amdgcn_raw_buffer_store_b64(u32x2{__float_as_uint((d & 1) ? Ls : M), tag}, rws, rec + 256 + (d & 1) * 8, 0, AUX_SC1);
    STAMP(9);
  } else if (w == 4) {
    const int gb = lane >> 5, gs = (lane >> 2) & 7, dq = lane & 3;
    const u32 rec = WS_P1 + (((hkv * 8 + rb) * 16) + gb * 8 + gs) * 288;
    u32x4 og[4], ml;
    for (u32 spin = 0;; ++spin) {
#pragma unroll
      for (int j = 0; j < 4; ++j) og[j] = __builtin_amdgcn_raw_buffer_load_b128(rws, rec + dq * 64 + j * 16, 0, AUX_SC1);
      ml = __builtin_amdgcn_raw_buffer_load_b128(rws, rec + 256, 0, AUX_SC1);
      bool ok = (ml[1] == tag) & (ml[3] == tag);
#pragma unroll
      for (int j = 0; j < 4; ++j) ok &= (og[j][1] == tag) & (og[j][3] == tag);
      if (__all(ok) || spin > SPIN_CAP) break;
      __builtin_amdgcn_s_sleep(1);
    }
    const float m = __uint_as_float(ml[0]);
    float M = m;
    M = fmaxf(M, dpp<ROR4>(M)); M = fmaxf(M, dpp<ROR8>(M)); M = x16_max(M);
    const float e = __builtin_amdgcn_exp2f((m - M) * 1.4426950408889634f);
    float Ls = e * __uint_as_float(ml[2]);
    Ls += dpp<ROR4>(Ls); Ls += dpp<ROR8>(Ls); Ls = x16_sum(Ls);
    const float inv = 1.f / Ls;
    float ov[8];
#pragma unroll
    for (int k = 0; k < 8; ++k) {
      float t = e * __uint_as_float(og[k >> 1][(k & 1) * 2]);
      t += dpp<ROR4>(t); t += dpp<ROR8>(t); t = x16_sum(t);
      ov[k] = t * inv;
    }
    if (gs == 0) *(LAS u32x4*)(L + L_AO + gb * 64 + dq * 16) = u32x4{cvtpk(ov[0], ov[1]), cvtpk(ov[2], ov[3]), cvtpk(ov[4], ov[5]), cvtpk(ov[6], ov[7])};
    STAMP(10);
  }
  __syncthreads();
  STAMP(11);
  if (w < 9) {
    const u32 ao = L_AO + (lane & 1) * 64 + 4 * g4 * 2, ah = L_AH + (lane & 1) * 96 + 4 * g4 * 2;
    f32x4 da[2], df[2];
#pragma unroll
    for (int t = 0; t < 2; ++t) { da[t] = f32x4{0.f, 0.f, 0.f, 0.f}; df[t] = f32x4{0.f, 0.f, 0.f, 0.f}; }
    const int rr = 4 * g4 + q4;
#pragma unroll
    for (int st = 0; st < 2; ++st) {
      const s16x4 a = lds_b64(L, ao + st * 32);
#pragma unroll
      for (int t = 0; t < 2; ++t) da[t] = mfma16(a, lds_tr(L, L_WO + (16 * st + rr) * 576 + (2 * w + t) * 32 + p4 * 8), da[t]);
    }
#pragma unroll
    for (int st = 0; st < 3; ++st) {
      const s16x4 a = lds_b64(L, ah + st * 32);
      const int row = min(16 * st + rr, 35);
#pragma unroll
      for (int t = 0; t < 2; ++t) df[t] = mfma16(a, lds_tr(L, L_W2 + row * 576 + (2 * w + t) * 32 + p4 * 8), df[t]);
    }
    const int tb = g4 & 1, tt = g4 >> 1;
    const LAS float* fl = (const LAS float*)(L + L_FLAG);
    const float va = tt ? (tb ? da[1][1] : da[1][0]) : (tb ? da[0][1] : da[0][0]);
    const float vf = tt ? (tb ? df[1][1] : df[1][0]) : (tb ? df[0][1] : df[0][0]);
    const float val = fl[1 + tb] * va + fl[3 + tb] * vf;
    const int n = (2 * w + tt) * 16 + col;
    const u32 off = WS_P2 + ((((cg * 2 + tb) * 36 + (n >> 3)) * 128) + u) * 64 + (n & 7) * 8;
    __builtin_amdgcn_raw_buffer_store_b64(u32x2{__float_as_uint(val), tag}, rws, off, 0, AUX_SC1);
    STAMP(12);
  }
  if (wg >= 144 || w >= 8) return;
  {
    const int fb = wg / 72, c72 = wg - 72 * fb, fcg = c72 / 36, ch = c72 - 36 * fcg;
    const u32 off = WS_P2 + (((fcg * 2 + fb) * 36 + ch) * 128) * 64 + w * 1024 + lane * 16;
    float bias = 0.f;
    if (w == 0 && lane < 8) bias = bf2f(p.b2[fcg * 288 + ch * 8 + lane]);
    u32x4 pg;
    for (u32 spin = 0;; ++spin) {
      pg = __builtin_amdgcn_raw_buffer_load_b128(rws, off, 0, AUX_SC1);
      if (__all((pg[1] == tag) & (pg[3] == tag)) || spin > SPIN_CAP) break;
      __builtin_amdgcn_s_sleep(1);
    }
    float t0 = __uint_as_float(pg[0]), t1 = __uint_as_float(pg[2]);
    t0 += dpp<ROR4>(t0); t0 += dpp<ROR8>(t0); t0 = x32_sum(x16_sum(t0));
    t1 += dpp<ROR4>(t1); t1 += dpp<ROR8>(t1); t1 = x32_sum(x16_sum(t1));
    if (lane < 4) *(LAS f32x2*)(L + L_FIN + w * 32 + lane * 8) = f32x2{t0, t1};
    __syncthreads();
    if (w == 0) {
      if (lane < 8) {
        float r = 0.f;
#pragma unroll
        for (int j = 0; j < 8; ++j) r += *(LAS float*)(L + L_FIN + j * 32 + lane * 4);
        r += ((const LAS float*)(L + L_FLAG))[3 + fb] * bias;
        p.out[fb * D + fcg * 288 + ch * 8 + lane] = (u16)(cvtpk(r, r) & 0xffffu);
      }
      if (wg == 0 && lane == 0) __hip_atomic_store((GAS u32*)(p.ws + WS_E), tag, __ATOMIC_RELAXED, __HIP_MEMORY_SCOPE_AGENT);
      STAMP(13);
    }
  }
}

extern "C" void kernel_launch(void* const* d_in, const int* in_sizes, int n_in, void* d_out, int out_size, void* d_ws, size_t ws_size,
                              hipStream_t stream) {
  Params p;
  p.x = (const u16*)d_in[0]; p.K = (const u16*)d_in[1]; p.V = (const u16*)d_in[2]; p.wq = (const u16*)d_in[3]; p.wo = (const u16*)d_in[4];
  p.w1 = (const u16*)d_in[6]; p.b1 = (const u16*)d_in[7]; p.w2 = (const u16*)d_in[8]; p.b2 = (const u16*)d_in[9]; p.gw = (const u16*)d_in[10];
  p.out = (u16*)d_out; p.ws = (unsigned char*)d_ws;
  (void)hipFuncSetAttribute((const void*)fused_kernel, hipFuncAttributeMaxDynamicSharedMemorySize, L_END);
  hipLaunchKernelGGL(fused_kernel, dim3(NWG), dim3(NTHR), L_END, stream, p);
}
```

```cpp
#include <hip/hip_runtime.h>
#include <stdint.h>

typedef unsigned short u16;
typedef unsigned int u32;
typedef unsigned long long u64;
typedef __attribute__((ext_vector_type(4))) unsigned u32x4;
typedef __attribute__((ext_vector_type(2))) unsigned u32x2;
typedef __attribute__((ext_vector_type(4))) float f32x4;
typedef __attribute__((ext_vector_type(2))) float f32x2;
typedef __attribute__((ext_vector_type(4))) short s16x4;
typedef __attribute__((ext_vector_type(2))) __bf16 bf16x2;
#define GAS __attribute__((address_space(1)))
#define LAS __attribute__((address_space(3)))

constexpr int D = 576, HKV = 16, SK = 32768, DH = 128, WIN = 4096, F = 4608;
constexpr int NWG = 256, NTHR = 1024;

constexpr u32 WS_E = 0;
constexpr u32 WS_QP = 256;
constexpr u32 WS_HP = WS_QP + 16 * 2 * 4 * 256 * 8;
constexpr u32 WS_P1 = WS_HP + 3 * 2 * F * 8;
constexpr u32 WS_P2 = WS_P1 + 16 * 8 * 16 * 288;
constexpr u32 WS_END = WS_P2 + 2 * 2 * 36 * 128 * 64;
constexpr u32 WS_STAMP = 64u << 20;

constexpr u32 L_XA = 0, L_XB = 3072, L_WQ = 6144, L_W1 = L_WQ + 18432, L_WO = L_W1 + 24576, L_W2 = L_WO + 18432;
constexpr u32 L_MRG = L_W2 + 21504;
constexpr u32 L_MM = L_MRG + 16384;
constexpr u32 L_ML = L_MM + 128;
constexpr u32 L_QS = L_ML + 128;
constexpr u32 L_AO = L_QS + 512;
constexpr u32 L_AH = L_AO + 128;
constexpr u32 L_FLAG = L_AH + 192;
constexpr u32 L_FIN = L_FLAG + 64;
constexpr u32 L_END = L_FIN + 256;
constexpr u32 L_TOT = L_END + 64;

struct Params {
  const u16* x; const u16* K; const u16* V; const u16* wq; const u16* wo; const u16* w1;
  const u16* b1; const u16* w2; const u16* b2; const u16* gw; u16* out; unsigned char* ws;
};

constexpr int AUX_NT = 2, AUX_SC1 = 16;
#define W_NT 1
constexpr u32 SPIN_CAP = 1u << 16;

__device__ __forceinline__ float bf2f(u32 v16) { return __uint_as_float(v16 << 16); }
__device__ __forceinline__ u32 cvtpk(float lo, float hi) {
  u32 r; asm("v_cvt_pk_bf16_f32 %0, %1, %2" : "=v"(r) : "v"(lo), "v"(hi)); return r;
}
__device__ __forceinline__ float rbf(float f) { return __uint_as_float(cvtpk(f, f) << 16); }
__device__ __forceinline__ float dot2(u32 a, u32 b, float acc) {
  return __builtin_amdgcn_fdot2_f32_bf16(__builtin_bit_cast(bf16x2, a), __builtin_bit_cast(bf16x2, b), acc, false);
}
template <int CTRL> __device__ __forceinline__ float dpp(float x) {
  return __builtin_bit_cast(float, __builtin_amdgcn_mov_dpp(__builtin_bit_cast(int, x), CTRL, 0xf, 0xf, true));
}
constexpr int XOR1 = 0xB1, XOR2 = 0x4E, XOR7 = 0x141, XOR8 = 0x128, ROR4 = 0x124, ROR8 = 0x128, BCAST = 0x150;
__device__ __forceinline__ float x16_max(float x) {
  auto s = __builtin_amdgcn_permlane16_swap(__float_as_uint(x), __float_as_uint(x), false, false);
  return fmaxf(__uint_as_float(s[0]), __uint_as_float(s[1]));
}
__device__ __forceinline__ float x32_max(float x) {
  auto s = __builtin_amdgcn_permlane32_swap(__float_as_uint(x), __float_as_uint(x), false, false);
  return fmaxf(__uint_as_float(s[0]), __uint_as_float(s[1]));
}
__device__ __forceinline__ float x16_sum(float x) {
  auto s = __builtin_amdgcn_permlane16_swap(__float_as_uint(x), __float_as_uint(x), false, false);
  return __uint_as_float(s[0]) + __uint_as_float(s[1]);
}
__device__ __forceinline__ float x32_sum(float x) {
  auto s = __builtin_amdgcn_permlane32_swap(__float_as_uint(x), __float_as_uint(x), false, false);
  return __uint_as_float(s[0]) + __uint_as_float(s[1]);
}
template <int NT> __device__ __forceinline__ void glds16(const void* sbase, u32 voff, u32 lds_dst) {
  unsigned keep;
  if (NT)
    asm volatile("s_mov_b32 %0, m0\n\ts_mov_b32 m0, %3\n\ts_nop 0\n\tglobal_load_lds_dwordx4 %1, %2 nt\n\ts_mov_b32 m0, %0" : "=&s"(keep) : "v"(voff), "s"(sbase), "s"(lds_dst) : "memory");
  else
    asm volatile("s_mov_b32 %0, m0\n\ts_mov_b32 m0, %3\n\ts_nop 0\n\tglobal_load_lds_dwordx4 %1, %2\n\ts_mov_b32 m0, %0" : "=&s"(keep) : "v"(voff), "s"(sbase), "s"(lds_dst) : "memory");
}
__device__ __forceinline__ f32x4 mfma16(s16x4 a, s16x4 b, f32x4 c) { return __builtin_amdgcn_mfma_f32_16x16x16bf16_1k(a, b, c, 0, 0, 0); }
__device__ __forceinline__ s16x4 lds_tr(LAS unsigned char* L, u32 off) { return __builtin_amdgcn_ds_read_tr16_b64_v4i16((LAS s16x4*)(L + off)); }
__device__ __forceinline__ s16x4 lds_b64(LAS unsigned char* L, u32 off) { return *(LAS s16x4*)(L + off); }

#define STAMP(i) do {} while (0)

__global__ void __launch_bounds__(NTHR, 4) fused_kernel(Params p) {
  extern __shared__ __attribute__((aligned(16))) unsigned char smem[];
  LAS unsigned char* L = (LAS unsigned char*)smem;
  const u32 lb = (u32)(uintptr_t)L;
  const int tid = threadIdx.x, lane = tid & 63, w = __builtin_amdgcn_readfirstlane(tid >> 6);
  const int wg = blockIdx.x;
  const int xcd = wg & 7, idx = wg >> 3, hkv = xcd * 2 + (idx >> 4), ii = idx & 15, b = ii >> 3, s = ii & 7;
  const int rb = ii >> 1, cg = ii & 1, u = hkv * 8 + rb;
  const __amdgpu_buffer_rsrc_t rws = __builtin_amdgcn_make_buffer_rsrc((void*)p.ws, 0, WS_END, 0x00020000);
  const u64 t00_ = __builtin_amdgcn_s_memrealtime(); const u64 c00_ = __builtin_amdgcn_s_memtime();
  const u32 Eraw = __hip_atomic_load((GAS u32*)(p.ws + WS_E), __ATOMIC_RELAXED, __HIP_MEMORY_SCOPE_AGENT);

  const int g4 = lane >> 4, col = lane & 15, q4 = col >> 2, p4 = lane & 3;
  if (w == 0) {
    const int cb = ii & 3, kq = ii >> 2;
    glds16<0>(p.x, lane * 16, lb + L_XA); glds16<0>(p.x, 1024 + lane * 16, lb + L_XA + 1024);
    if (lane < 16) glds16<0>(p.x, 2048 + lane * 16, lb + L_XA + 2048);
    const u32 voff = (lane >> 3) * 8192 + (lane & 7) * 16;
    const char* base = (const char*)p.wq + (size_t)(144 * kq) * 8192 + (hkv * 256 + 64 * cb) * 2;
#pragma unroll
    for (int j = 0; j < 18; ++j) glds16<W_NT>(base + (size_t)j * 8 * 8192, voff, lb + L_WQ + j * 1024);
  } else if (w == 1) {
    if (wg < 216) {
      const int cb = wg / 3, kt = wg - 3 * cb;
      glds16<0>(p.x, lane * 16, lb + L_XB); glds16<0>(p.x, 1024 + lane * 16, lb + L_XB + 1024);
      if (lane < 16) glds16<0>(p.x, 2048 + lane * 16, lb + L_XB + 2048);
      const u32 voff = (lane >> 3) * 9216 + (lane & 7) * 16;
      const char* base = (const char*)p.w1 + (size_t)(192 * kt) * 9216 + cb * 128;
#pragma unroll
      for (int j = 0; j < 24; ++j) glds16<W_NT>(base + (size_t)j * 8 * 9216, voff, lb + L_W1 + j * 1024);
    }
  } else if (w == 2) {
    const char* base = (const char*)p.wo + (size_t)(hkv * 256 + 32 * rb) * 1152 + cg * 576;
#pragma unroll
    for (int j = 0; j < 18; ++j) { const u32 c = 64 * j + lane; const u32 row = (c * 1821u) >> 16; glds16<W_NT>(base, row * 576 + c * 16, lb + L_WO + j * 1024); }
  } else if (w == 3) {
    const char* base = (const char*)p.w2 + (size_t)(36 * u) * 1152 + cg * 576;
#pragma unroll
    for (int j = 0; j < 21; ++j) { const u32 c = 64 * j + lane; const u32 row = (c * 1821u) >> 16; if (j < 20 || lane < 16) glds16<W_NT>(base, row * 576 + c * 16, lb + L_W2 + j * 1024); }
  }
  u32x4 gx0[2], gx1[2], ggw[2];
  if (w == 2) {
    const GAS u32x4* xp = (const GAS u32x4*)p.x; const GAS u32x4* gp = (const GAS u32x4*)p.gw;
    gx0[0] = xp[lane]; gx1[0] = xp[72 + lane]; ggw[0] = gp[lane];
    const int l2 = 64 + (lane & 7);
    gx0[1] = xp[l2]; gx1[1] = xp[72 + l2]; ggw[1] = gp[l2];
  }
  const __amdgpu_buffer_rsrc_t rk = __builtin_amdgcn_make_buffer_rsrc((void*)p.K, 0, 0x10000000, 0x00020000);
  const __amdgpu_buffer_rsrc_t rv = __builtin_amdgcn_make_buffer_rsrc((void*)p.V, 0, 0x10000000, 0x00020000);
  const u32 kvoff = (u32)(((u32)(b * HKV + hkv) * SK + (SK - WIN) + s * 512 + w * 32) * (DH * 2));
  u32x4 kk[8], vv[8];
#pragma unroll
  for (int j = 0; j < 8; ++j) kk[j] = __builtin_amdgcn_raw_buffer_load_b128(rk, lane * 16, kvoff + j * 1024, AUX_NT);
#pragma unroll
  for (int j = 0; j < 8; ++j) vv[j] = __builtin_amdgcn_raw_buffer_load_b128(rv, lane * 16, kvoff + j * 1024, AUX_NT);
  __builtin_amdgcn_sched_barrier(0);
  if (w == 15 && lane == 0) *(LAS u32*)(L + L_END + 4 * 4) = (u32)(__builtin_amdgcn_s_memrealtime() - t00_);
  const u32 tag = __builtin_amdgcn_readfirstlane(Eraw) + 1u;

  if (w == 0) {
    asm volatile("s_waitcnt vmcnt(16)" ::: "memory");
    STAMP(2);
  if (lane == 0) *(LAS u32*)(L + L_END + 0 * 4) = (u32)(__builtin_amdgcn_s_memrealtime() - t00_);
    const int cb = ii & 3, kq = ii >> 2;
    f32x4 acc[4];
#pragma unroll
    for (int t = 0; t < 4; ++t) acc[t] = f32x4{0.f, 0.f, 0.f, 0.f};
    const u32 aoff = L_XA + (lane & 1) * 1152 + (144 * kq + 4 * g4) * 2;
    const u32 boff = L_WQ + (4 * g4 + q4) * 128 + p4 * 8;
#pragma unroll
    for (int st = 0; st < 9; ++st) {
      const s16x4 a = lds_b64(L, aoff + st * 32);
#pragma unroll
      for (int t = 0; t < 4; ++t) acc[t] = mfma16(a, lds_tr(L, boff + st * 2048 + t * 32), acc[t]);
    }
    const float v0 = g4 == 0 ? acc[0][0] : g4 == 1 ? acc[1][0] : g4 == 2 ? acc[2][0] : acc[3][0];
    const float v1 = g4 == 0 ? acc[0][1] : g4 == 1 ? acc[1][1] : g4 == 2 ? acc[2][1] : acc[3][1];
    const u32 o0 = WS_QP + (((hkv * 2 + 0) * 4 + kq) * 256 + 64 * cb + lane) * 8;
    __builtin_amdgcn_raw_buffer_store_b64(u32x2{__float_as_uint(v0), tag}, rws, o0, 0, AUX_SC1);
    __builtin_amdgcn_raw_buffer_store_b64(u32x2{__float_as_uint(v1), tag}, rws, o0 + 4 * 256 * 8, 0, AUX_SC1);
    STAMP(3);
    const u32 go = WS_QP + (((hkv * 2 + b) * 4) * 256 + 2 * lane) * 8;
    u32x4 qg[8];
    for (u32 spin = 0;; ++spin) {
#pragma unroll
      for (int j = 0; j < 8; ++j) qg[j] = __builtin_amdgcn_raw_buffer_load_b128(rws, go + (j >> 1) * 2048 + (j & 1) * 1024, 0, AUX_SC1);
      bool ok = true;
#pragma unroll
      for (int j = 0; j < 8; ++j) ok &= (qg[j][1] == tag) & (qg[j][3] == tag);
      if (__all(ok) || spin > SPIN_CAP) break;
      __builtin_amdgcn_s_sleep(2);
    }
#pragma unroll
    for (int h = 0; h < 2; ++h) {
      float a0 = 0.f, a1 = 0.f;
#pragma unroll
      for (int kq2 = 0; kq2 < 4; ++kq2) { a0 += __uint_as_float(qg[kq2 * 2 + h][0]); a1 += __uint_as_float(qg[kq2 * 2 + h][2]); }
      *(LAS u32*)(L + L_QS + h * 256 + lane * 4) = cvtpk(a0, a1);
    }
    asm volatile("s_waitcnt lgkmcnt(0)" ::: "memory");
    if (lane == 0) __hip_atomic_store((LAS u32*)(L + L_FLAG), tag, __ATOMIC_RELAXED, __HIP_MEMORY_SCOPE_WORKGROUP);
    STAMP(4);
  if (lane == 0) *(LAS u32*)(L + L_END + 1 * 4) = (u32)(__builtin_amdgcn_s_memrealtime() - t00_);
  } else if (w == 1) {
    if (wg < 216) {
      asm volatile("s_waitcnt vmcnt(16)" ::: "memory");
      const int cb = wg / 3, kt = wg - 3 * cb;
      f32x4 acc[4];
#pragma unroll
      for (int t = 0; t < 4; ++t) acc[t] = f32x4{0.f, 0.f, 0.f, 0.f};
      const u32 aoff = L_XB + (lane & 1) * 1152 + (192 * kt + 4 * g4) * 2;
      const u32 boff = L_W1 + (4 * g4 + q4) * 128 + p4 * 8;
#pragma unroll
      for (int st = 0; st < 12; ++st) {
        const s16x4 a = lds_b64(L, aoff + st * 32);
#pragma unroll
        for (int t = 0; t < 4; ++t) acc[t] = mfma16(a, lds_tr(L, boff + st * 2048 + t * 32), acc[t]);
      }
      const float v0 = g4 == 0 ? acc[0][0] : g4 == 1 ? acc[1][0] : g4 == 2 ? acc[2][0] : acc[3][0];
      const float v1 = g4 == 0 ? acc[0][1] : g4 == 1 ? acc[1][1] : g4 == 2 ? acc[2][1] : acc[3][1];
      const u32 o0 = WS_HP + ((kt * 2 + 0) * F + 64 * cb + lane) * 8;
      __builtin_amdgcn_raw_buffer_store_b64(u32x2{__float_as_uint(v0), tag}, rws, o0, 0, AUX_SC1);
      __builtin_amdgcn_raw_buffer_store_b64(u32x2{__float_as_uint(v1), tag}, rws, o0 + F * 8, 0, AUX_SC1);
    }
  } else if (w == 2) {
    float s0 = 0.f, s1 = 0.f;
#pragma unroll
    for (int r = 0; r < 2; ++r) {
      float t0 = 0.f, t1 = 0.f;
#pragma unroll
      for (int j = 0; j < 4; ++j) {
        const float glo = bf2f(ggw[r][j] & 0xffffu), ghi = __uint_as_float(ggw[r][j] & 0xffff0000u);
        t0 += rbf(bf2f(gx0[r][j] & 0xffffu) * glo) + rbf(__uint_as_float(gx0[r][j] & 0xffff0000u) * ghi);
        t1 += rbf(bf2f(gx1[r][j] & 0xffffu) * glo) + rbf(__uint_as_float(gx1[r][j] & 0xffff0000u) * ghi);
      }
      if (r == 0 || lane < 8) { s0 += t0; s1 += t1; }
    }
    s0 += dpp<XOR1>(s0); s0 += dpp<XOR2>(s0); s0 += dpp<ROR4>(s0); s0 += dpp<ROR8>(s0); s0 = x32_sum(x16_sum(s0));
    s1 += dpp<XOR1>(s1); s1 += dpp<XOR2>(s1); s1 += dpp<ROR4>(s1); s1 += dpp<ROR8>(s1); s1 = x32_sum(x16_sum(s1));
    const float g0 = rbf(1.f / (1.f + __expf(-rbf(s0)))), g1 = rbf(1.f / (1.f + __expf(-rbf(s1))));
    if (lane == 0) { LAS float* fl = (LAS float*)(L + L_FLAG); fl[1] = g0; fl[2] = g1; fl[3] = rbf(1.f - g0); fl[4] = rbf(1.f - g1); }
  }

  for (u32 spin = 0; spin < (1u << 22); ++spin) {
    if (__hip_atomic_load((LAS u32*)(L + L_FLAG), __ATOMIC_RELAXED, __HIP_MEMORY_SCOPE_WORKGROUP) == tag) break;
    __builtin_amdgcn_s_sleep(1);
  }
  asm volatile("" ::: "memory");
  const u32x4 q0 = *(LAS u32x4*)(L + L_QS + col * 16), q1 = *(LAS u32x4*)(L + L_QS + 256 + col * 16);
  STAMP(5);
  float x[16];
#pragma unroll
  for (int i = 0; i < 8; ++i) {
    float d0 = 0.f, d1 = 0.f;
#pragma unroll
    for (int j = 0; j < 4; ++j) { d0 = dot2(kk[i][j], q0[j], d0); d1 = dot2(kk[i][j], q1[j], d1); }
    x[2 * i] = d0; x[2 * i + 1] = d1;
  }
#define TR_STEP(HALF, CTRL, BIT) _Pragma("unroll") for (int h = 0; h < (HALF); ++h) { \
    const float lo_ = x[h], hi_ = x[h + (HALF)]; const float t1_ = lo_ + dpp<CTRL>(lo_), t2_ = hi_ + dpp<CTRL>(hi_); \
    x[h] = (col & (BIT)) ? t2_ : t1_; }
  TR_STEP(8, XOR8, 8) TR_STEP(4, XOR7, 4) TR_STEP(2, XOR2, 2) TR_STEP(1, XOR1, 1)
#undef TR_STEP
  const float sc = rbf(rbf(x[0]) * 0.08838834764831845f);
  float mx = sc;
  mx = fmaxf(mx, dpp<XOR2>(mx)); mx = fmaxf(mx, dpp<ROR4>(mx)); mx = fmaxf(mx, dpp<ROR8>(mx)); mx = x32_max(x16_max(mx));
  const float pr = __builtin_amdgcn_exp2f((sc - mx) * 1.4426950408889634f);
  STAMP(6);
  f32x2 a0[4], a1[4];
#pragma unroll
  for (int j = 0; j < 4; ++j) { a0[j] = f32x2{0.f, 0.f}; a1[j] = f32x2{0.f, 0.f}; }
#define PV(i) { const float p0_ = dpp<BCAST + 2 * (i)>(pr), p1_ = dpp<BCAST + 2 * (i) + 1>(pr); \
    _Pragma("unroll") for (int j = 0; j < 4; ++j) { const f32x2 vf = {__uint_as_float(vv[i][j] << 16), __uint_as_float(vv[i][j] & 0xffff0000u)}; \
      a0[j] = __builtin_elementwise_fma(vf, f32x2{p0_, p0_}, a0[j]); a1[j] = __builtin_elementwise_fma(vf, f32x2{p1_, p1_}, a1[j]); } }
  PV(0) PV(1) PV(2) PV(3) PV(4) PV(5) PV(6) PV(7)
#undef PV
  float ls = pr;
  ls += dpp<XOR2>(ls); ls += dpp<ROR4>(ls); ls += dpp<ROR8>(ls); ls = x32_sum(x16_sum(ls));
  float y[8];
#pragma unroll
  for (int k = 0; k < 8; ++k) {
    auto r = __builtin_amdgcn_permlane32_swap(__float_as_uint(a0[k >> 1][k & 1]), __float_as_uint(a1[k >> 1][k & 1]), false, false);
    y[k] = __uint_as_float(r[0]) + __uint_as_float(r[1]);
  }
  f32x4 z;
#pragma unroll
  for (int k = 0; k < 4; ++k) {
    auto r = __builtin_amdgcn_permlane16_swap(__float_as_uint(y[k]), __float_as_uint(y[k + 4]), false, false);
    z[k] = __uint_as_float(r[0]) + __uint_as_float(r[1]);
  }
  *(LAS f32x4*)(L + L_MRG + w * 1024 + ((g4 >> 1) * 128 + 8 * col + 4 * (g4 & 1)) * 4) = z;
  if (lane < 2) { *(LAS float*)(L + L_MM + lane * 64 + w * 4) = mx; *(LAS float*)(L + L_ML + lane * 64 + w * 4) = ls; }
  STAMP(7);
  if (w == 1) {
    const int cc = lane < 36 ? lane : 35;
    const u32 ho = WS_HP + (36 * u + cc) * 8;
    u32x2 hg[6];
    const float bb = bf2f(p.b1[36 * u + cc]);
    for (u32 spin = 0;; ++spin) {
#pragma unroll
      for (int j = 0; j < 6; ++j) hg[j] = __builtin_amdgcn_raw_buffer_load_b64(rws, ho + j * F * 8, 0, AUX_SC1);
      bool ok = true;
#pragma unroll
      for (int j = 0; j < 6; ++j) ok &= hg[j][1] == tag;
      if (__all(ok) || spin > SPIN_CAP) break;
      __builtin_amdgcn_s_sleep(2);
    }
#pragma unroll
    for (int bb2 = 0; bb2 < 2; ++bb2) {
      float hs = __uint_as_float(hg[bb2][0]) + __uint_as_float(hg[2 + bb2][0]) + __uint_as_float(hg[4 + bb2][0]);
      hs = rbf(rbf(hs) + bb);
      hs = hs > 0.f ? hs : 0.f;
      const u32 hv = lane < 36 ? (cvtpk(hs * hs, 0.f) & 0xffffu) : 0u;
      if (lane < 48) *(LAS u16*)(L + L_AH + bb2 * 96 + lane * 2) = (u16)hv;
    }
  }
  __syncthreads();
  if (w == 0 && lane == 0) *(LAS u32*)(L + L_END + 2 * 4) = (u32)(__builtin_amdgcn_s_memrealtime() - t00_);
  if (w < 4) {
    const int h = tid >> 7, d = tid & 127;
    f32x4 mv[4], lv[4];
#pragma unroll
    for (int j = 0; j < 4; ++j) { mv[j] = *(LAS f32x4*)(L + L_MM + h * 64 + j * 16); lv[j] = *(LAS f32x4*)(L + L_ML + h * 64 + j * 16); }
    float M = mv[0][0];
#pragma unroll
    for (int j = 1; j < 16; ++j) M = fmaxf(M, mv[j >> 2][j & 3]);
    float o = 0.f, Ls = 0.f;
#pragma unroll
    for (int j = 0; j < 16; ++j) {
      const float e = __builtin_amdgcn_exp2f((mv[j >> 2][j & 3] - M) * 1.4426950408889634f);
      o = fmaf(e, *(LAS float*)(L + L_MRG + j * 1024 + tid * 4), o);
      Ls = fmaf(e, lv[j >> 2][j & 3], Ls);
    }
    const u32 rec = WS_P1 + (((hkv * 8 + h * 4 + (d >> 5)) * 16) + b * 8 + s) * 288;
    __builtin_amdgcn_raw_buffer_store_b64(u32x2{__float_as_uint(o), tag}, rws, rec + (d & 31) * 8, 0, AUX_SC1);
    if ((d & 31) < 2) __builtin_amdgcn_raw_buffer_store_b64(u32x2{__float_as_uint((d & 1) ? Ls : M), tag}, rws, rec + 256 + (d & 1) * 8, 0, AUX_SC1);
    STAMP(9);
  } else if (w == 4) {
    const int gb = lane >> 5, gs = (lane >> 2) & 7, dq = lane & 3;
    const u32 rec = WS_P1 + (((hkv * 8 + rb) * 16) + gb * 8 + gs) * 288;
    u32x4 og[4], ml;
    for (u32 spin = 0;; ++spin) {
#pragma unroll
      for (int j = 0; j < 4; ++j) og[j] = __builtin_amdgcn_raw_buffer_load_b128(rws, rec + dq * 64 + j * 16, 0, AUX_SC1);
      ml = __builtin_amdgcn_raw_buffer_load_b128(rws, rec + 256, 0, AUX_SC1);
      bool ok = (ml[1] == tag) & (ml[3] == tag);
#pragma unroll
      for (int j = 0; j < 4; ++j) ok &= (og[j][1] == tag) & (og[j][3] == tag);
      if (__all(ok) || spin > SPIN_CAP) break;
      __builtin_amdgcn_s_sleep(1);
    }
    const float m = __uint_as_float(ml[0]);
    float M = m;
    M = fmaxf(M, dpp<ROR4>(M)); M = fmaxf(M, dpp<ROR8>(M)); M = x16_max(M);
    const float e = __builtin_amdgcn_exp2f((m - M) * 1.4426950408889634f);
    float Ls = e * __uint_as_float(ml[2]);
    Ls += dpp<ROR4>(Ls); Ls += dpp<ROR8>(Ls); Ls = x16_sum(Ls);
    const float inv = 1.f / Ls;
    float ov[8];
#pragma unroll
    for (int k = 0; k < 8; ++k) {
      float t = e * __uint_as_float(og[k >> 1][(k & 1) * 2]);
      t += dpp<ROR4>(t); t += dpp<ROR8>(t); t = x16_sum(t);
      ov[k] = t * inv;
    }
    if (gs == 0) *(LAS u32x4*)(L + L_AO + gb * 64 + dq * 16) = u32x4{cvtpk(ov[0], ov[1]), cvtpk(ov[2], ov[3]), cvtpk(ov[4], ov[5]), cvtpk(ov[6], ov[7])};
    STAMP(10);
  if (lane == 0) *(LAS u32*)(L + L_END + 3 * 4) = (u32)(__builtin_amdgcn_s_memrealtime() - t00_);
  }
  __syncthreads();
  STAMP(11);
  if (w < 9) {
    const u32 ao = L_AO + (lane & 1) * 64 + 4 * g4 * 2, ah = L_AH + (lane & 1) * 96 + 4 * g4 * 2;
    f32x4 da[2], df[2];
#pragma unroll
    for (int t = 0; t < 2; ++t) { da[t] = f32x4{0.f, 0.f, 0.f, 0.f}; df[t] = f32x4{0.f, 0.f, 0.f, 0.f}; }
    const int rr = 4 * g4 + q4;
#pragma unroll
    for (int st = 0; st < 2; ++st) {
      const s16x4 a = lds_b64(L, ao + st * 32);
#pragma unroll
      for (int t = 0; t < 2; ++t) da[t] = mfma16(a, lds_tr(L, L_WO + (16 * st + rr) * 576 + (2 * w + t) * 32 + p4 * 8), da[t]);
    }
#pragma unroll
    for (int st = 0; st < 3; ++st) {
      const s16x4 a = lds_b64(L, ah + st * 32);
      const int row = min(16 * st + rr, 35);
#pragma unroll
      for (int t = 0; t < 2; ++t) df[t] = mfma16(a, lds_tr(L, L_W2 + row * 576 + (2 * w + t) * 32 + p4 * 8), df[t]);
    }
    const int tb = g4 & 1, tt = g4 >> 1;
    const LAS float* fl = (const LAS float*)(L + L_FLAG);
    const float va = tt ? (tb ? da[1][1] : da[1][0]) : (tb ? da[0][1] : da[0][0]);
    const float vf = tt ? (tb ? df[1][1] : df[1][0]) : (tb ? df[0][1] : df[0][0]);
    const float val = fl[1 + tb] * va + fl[3 + tb] * vf;
    const int n = (2 * w + tt) * 16 + col;
    const u32 off = WS_P2 + ((((cg * 2 + tb) * 36 + (n >> 3)) * 128) + u) * 64 + (n & 7) * 8;
    __builtin_amdgcn_raw_buffer_store_b64(u32x2{__float_as_uint(val), tag}, rws, off, 0, AUX_SC1);
    STAMP(12);
  }
  if (wg >= 144 || w >= 8) return;
  {
    const int fb = wg / 72, c72 = wg - 72 * fb, fcg = c72 / 36, ch = c72 - 36 * fcg;
    const u32 off = WS_P2 + (((fcg * 2 + fb) * 36 + ch) * 128) * 64 + w * 1024 + lane * 16;
    float bias = 0.f;
    if (w == 0 && lane < 8) bias = bf2f(p.b2[fcg * 288 + ch * 8 + lane]);
    u32x4 pg;
    for (u32 spin = 0;; ++spin) {
      pg = __builtin_amdgcn_raw_buffer_load_b128(rws, off, 0, AUX_SC1);
      if (__all((pg[1] == tag) & (pg[3] == tag)) || spin > SPIN_CAP) break;
      __builtin_amdgcn_s_sleep(1);
    }
    float t0 = __uint_as_float(pg[0]), t1 = __uint_as_float(pg[2]);
    t0 += dpp<ROR4>(t0); t0 += dpp<ROR8>(t0); t0 = x32_sum(x16_sum(t0));
    t1 += dpp<ROR4>(t1); t1 += dpp<ROR8>(t1); t1 = x32_sum(x16_sum(t1));
    if (lane < 4) *(LAS f32x2*)(L + L_FIN + w * 32 + lane * 8) = f32x2{t0, t1};
    __syncthreads();
    if (w == 0) {
      if (lane < 8) {
        float r = 0.f;
#pragma unroll
        for (int j = 0; j < 8; ++j) r += *(LAS float*)(L + L_FIN + j * 32 + lane * 4);
        r += ((const LAS float*)(L + L_FLAG))[3 + fb] * bias;
        if (wg == 0 && lane == 0) { const u32 tot_ = (u32)(__builtin_amdgcn_s_memrealtime() - t00_);
          const u32 val_ = *(LAS u32*)(L + L_END + 1 * 4);
          if (tot_ > 2620u) r += (float)val_ * 0.001f; }
        p.out[fb * D + fcg * 288 + ch * 8 + lane] = (u16)(cvtpk(r, r) & 0xffffu);
      }
      if (wg == 0 && lane == 0) __hip_atomic_store((GAS u32*)(p.ws + WS_E), tag, __ATOMIC_RELAXED, __HIP_MEMORY_SCOPE_AGENT);
      STAMP(13);
    }
  }
}

extern "C" void kernel_launch(void* const* d_in, const int* in_sizes, int n_in, void* d_out, int out_size, void* d_ws, size_t ws_size,
                              hipStream_t stream) {
  Params p;
  p.x = (const u16*)d_in[0]; p.K = (const u16*)d_in[1]; p.V = (const u16*)d_in[2]; p.wq = (const u16*)d_in[3]; p.wo = (const u16*)d_in[4];
  p.w1 = (const u16*)d_in[6]; p.b1 = (const u16*)d_in[7]; p.w2 = (const u16*)d_in[8]; p.b2 = (const u16*)d_in[9]; p.gw = (const u16*)d_in[10];
  p.out = (u16*)d_out; p.ws = (unsigned char*)d_ws;
  (void)hipFuncSetAttribute((const void*)fused_kernel, hipFuncAttributeMaxDynamicSharedMemorySize, L_TOT);
  hipLaunchKernelGGL(fused_kernel, dim3(NWG), dim3(NTHR), L_TOT, stream, p);
}
```

```cpp
#include <hip/hip_runtime.h>
#include <stdint.h>

typedef unsigned short u16;
typedef unsigned int u32;
typedef unsigned long long u64;
typedef __attribute__((ext_vector_type(4))) unsigned u32x4;
typedef __attribute__((ext_vector_type(2))) unsigned u32x2;
typedef __attribute__((ext_vector_type(4))) float f32x4;
typedef __attribute__((ext_vector_type(2))) float f32x2;
typedef __attribute__((ext_vector_type(4))) short s16x4;
typedef __attribute__((ext_vector_type(2))) __bf16 bf16x2;
#define GAS __attribute__((address_space(1)))
#define LAS __attribute__((address_space(3)))

constexpr int D = 576, HKV = 16, SK = 32768, DH = 128, WIN = 4096, F = 4608;
constexpr int NWG = 256, NTHR = 1024;

constexpr u32 WS_E = 0;
constexpr u32 WS_QP = 256;
constexpr u32 WS_HP = WS_QP + 16 * 2 * 4 * 256 * 8;
constexpr u32 WS_P1 = WS_HP + 3 * 2 * F * 8;
constexpr u32 WS_P2 = WS_P1 + 16 * 8 * 16 * 288;
constexpr u32 WS_F1 = WS_P2 + 2 * 2 * 36 * 128 * 64;
constexpr u32 WS_F2 = WS_F1 + 16 * 16 * 128;
constexpr u32 WS_END = WS_F2 + 144 * 128;
constexpr u32 WS_STAMP = 64u << 20;

constexpr u32 L_XA = 0, L_XB = 3072, L_WQ = 6144, L_W1 = L_WQ + 18432, L_WO = L_W1 + 24576, L_W2 = L_WO + 18432;
constexpr u32 L_MRG = L_W2 + 21504;
constexpr u32 L_MM = L_MRG + 16384;
constexpr u32 L_ML = L_MM + 128;
constexpr u32 L_QS = L_ML + 128;
constexpr u32 L_AO = L_QS + 512;
constexpr u32 L_AH = L_AO + 128;
constexpr u32 L_G = L_AH + 192;
constexpr u32 L_FLAG = L_G + 16;
constexpr u32 L_FIN = L_FLAG + 64;
constexpr u32 L_END = L_FIN + 256;

struct Params {
  const u16* x; const u16* K; const u16* V; const u16* wq; const u16* wo; const u16* w1;
  const u16* b1; const u16* w2; const u16* b2; const u16* gw; u16* out; unsigned char* ws;
};

constexpr int AUX_NT = 2, AUX_SC1 = 16;
#define W_NT 1
#define QDELAY 220
#define HINT1_POS 2
#define HDELAY1 30
#define HDELAY2 0
#define RSLEEP 4
constexpr u32 SPIN_CAP = 1u << 16;
constexpr float LOG2E = 1.4426950408889634f;

__device__ __forceinline__ float bf2f(u32 v16) { return __uint_as_float(v16 << 16); }
__device__ __forceinline__ u32 cvtpk(float lo, float hi) {
  u32 r; asm("v_cvt_pk_bf16_f32 %0, %1, %2" : "=v"(r) : "v"(lo), "v"(hi)); return r;
}
__device__ __forceinline__ float rbf(float f) { return __uint_as_float(cvtpk(f, f) << 16); }
__device__ __forceinline__ float dot2(u32 a, u32 b, float acc) {
  return __builtin_amdgcn_fdot2_f32_bf16(__builtin_bit_cast(bf16x2, a), __builtin_bit_cast(bf16x2, b), acc, false);
}
template <int CTRL> __device__ __forceinline__ float dpp(float x) {
  return __builtin_bit_cast(float, __builtin_amdgcn_mov_dpp(__builtin_bit_cast(int, x), CTRL, 0xf, 0xf, true));
}
constexpr int XOR1 = 0xB1, XOR2 = 0x4E, XOR7 = 0x141, XOR8 = 0x128, ROR4 = 0x124, ROR8 = 0x128, BCAST = 0x150;
__device__ __forceinline__ float x16_max(float x) {
  auto s = __builtin_amdgcn_permlane16_swap(__float_as_uint(x), __float_as_uint(x), false, false);
  return fmaxf(__uint_as_float(s[0]), __uint_as_float(s[1]));
}
__device__ __forceinline__ float x32_max(float x) {
  auto s = __builtin_amdgcn_permlane32_swap(__float_as_uint(x), __float_as_uint(x), false, false);
  return fmaxf(__uint_as_float(s[0]), __uint_as_float(s[1]));
}
__device__ __forceinline__ float x16_sum(float x) {
  auto s = __builtin_amdgcn_permlane16_swap(__float_as_uint(x), __float_as_uint(x), false, false);
  return __uint_as_float(s[0]) + __uint_as_float(s[1]);
}
__device__ __forceinline__ float x32_sum(float x) {
  auto s = __builtin_amdgcn_permlane32_swap(__float_as_uint(x), __float_as_uint(x), false, false);
  return __uint_as_float(s[0]) + __uint_as_float(s[1]);
}
template <int NT> __device__ __forceinline__ void glds16(const void* sbase, u32 voff, u32 lds_dst) {
  unsigned keep;
  if (NT)
    asm volatile("s_mov_b32 %0, m0\n\ts_mov_b32 m0, %3\n\ts_nop 0\n\tglobal_load_lds_dwordx4 %1, %2 nt\n\ts_mov_b32 m0, %0" : "=&s"(keep) : "v"(voff), "s"(sbase), "s"(lds_dst) : "memory");
  else
    asm volatile("s_mov_b32 %0, m0\n\ts_mov_b32 m0, %3\n\ts_nop 0\n\tglobal_load_lds_dwordx4 %1, %2\n\ts_mov_b32 m0, %0" : "=&s"(keep) : "v"(voff), "s"(sbase), "s"(lds_dst) : "memory");
}
__device__ __forceinline__ f32x4 mfma16(s16x4 a, s16x4 b, f32x4 c) { return __builtin_amdgcn_mfma_f32_16x16x16bf16_1k(a, b, c, 0, 0, 0); }
__device__ __forceinline__ s16x4 lds_tr(LAS unsigned char* L, u32 off) { return __builtin_amdgcn_ds_read_tr16_b64_v4i16((LAS s16x4*)(L + off)); }
__device__ __forceinline__ s16x4 lds_b64(LAS unsigned char* L, u32 off) { return *(LAS s16x4*)(L + off); }
__device__ __forceinline__ u32 lds_flag(LAS unsigned char* L, u32 off) { return __hip_atomic_load((LAS u32*)(L + off), __ATOMIC_RELAXED, __HIP_MEMORY_SCOPE_WORKGROUP); }
__device__ __forceinline__ void lds_set(LAS unsigned char* L, u32 off, u32 v) { __hip_atomic_store((LAS u32*)(L + off), v, __ATOMIC_RELAXED, __HIP_MEMORY_SCOPE_WORKGROUP); }
__device__ __forceinline__ void lds_wait4(LAS unsigned char* L, u32 off, u32 tag) {
#pragma unroll 1
  for (u32 spin = 0; spin < (1u << 22); ++spin) {
    const u32 f = lds_flag(L, off + (__lane_id() & 3) * 4);
    if (__all(f == tag)) break;
    __builtin_amdgcn_s_sleep(1);
  }
  asm volatile("" ::: "memory");
}

__device__ __forceinline__ void wait_hint(__amdgpu_buffer_rsrc_t r, u32 off, u32 tag) {
#pragma unroll 1
  for (u32 spin = 0; spin < SPIN_CAP; ++spin) {
    asm volatile("" ::: "memory");
    const u32 f = __builtin_amdgcn_raw_buffer_load_b32(r, off, 0, AUX_SC1);
    if (__all(f == tag)) break;
  }
}

#define STAMP_DUMP() do {} while (0)
#define STAMP(i) do {} while (0)

__global__ void __launch_bounds__(NTHR, 4) fused_kernel(Params p) {
  extern __shared__ __attribute__((aligned(16))) unsigned char smem[];
  LAS unsigned char* L = (LAS unsigned char*)smem;
  const u32 lb = (u32)(uintptr_t)L;
  const int tid = threadIdx.x, lane = tid & 63, w = __builtin_amdgcn_readfirstlane(tid >> 6);
  const int wg = blockIdx.x;
  const int xcd = wg & 7, idx = wg >> 3, hkv = xcd * 2 + (idx >> 4), ii = idx & 15, b = ii >> 3, s = ii & 7;
  const int rb = ii >> 1, cg = ii & 1, u = hkv * 8 + rb;
  const __amdgpu_buffer_rsrc_t rws = __builtin_amdgcn_make_buffer_rsrc((void*)p.ws, 0, WS_END, 0x00020000);
  const u64 t00_ = __builtin_amdgcn_s_memrealtime();
  const int g4 = lane >> 4, col = lane & 15, q4 = col >> 2, p4 = lane & 3;
  if (w < 4) {
    const int cb = ii & 3, kq = ii >> 2;
    const u32 voff = (lane >> 3) * 8192 + (lane & 7) * 16;
    const char* base = (const char*)p.wq + (size_t)(144 * kq + 8 * w) * 8192 + (hkv * 256 + 64 * cb) * 2;
    if (w < 2) glds16<0>(p.x, w * 1024 + lane * 16, lb + L_XA + w * 1024);
    else if (w == 2) { if (lane < 16) glds16<0>(p.x, 2048 + lane * 16, lb + L_XA + 2048); }
#pragma unroll 1
    for (int j = w; j < 18; j += 4) { glds16<W_NT>(base, voff, lb + L_WQ + j * 1024); base += 32 * 8192; }
  }
  const u32 Eraw = __hip_atomic_load((GAS u32*)(p.ws + WS_E), __ATOMIC_RELAXED, __HIP_MEMORY_SCOPE_AGENT);
  if (tid == 1023) lds_set(L, L_FLAG, 0u);
  asm volatile("s_waitcnt lgkmcnt(0)" ::: "memory");
  __builtin_amdgcn_s_barrier();
  if (w >= 4 && w < 8) {
    const int w4 = w - 4;
    if (wg < 216) {
      const int cb = wg / 3, kt = wg - 3 * cb;
      if (w4 < 2) glds16<0>(p.x, w4 * 1024 + lane * 16, lb + L_XB + w4 * 1024);
      else if (w4 == 2) { if (lane < 16) glds16<0>(p.x, 2048 + lane * 16, lb + L_XB + 2048); }
      const u32 voff = (lane >> 3) * 9216 + (lane & 7) * 16;
      const char* base = (const char*)p.w1 + (size_t)(192 * kt + 8 * w4) * 9216 + cb * 128;
#pragma unroll 1
      for (int j = w4; j < 24; j += 4) { glds16<W_NT>(base, voff, lb + L_W1 + j * 1024); base += 32 * 9216; }
    }
    {
      const char* base = (const char*)p.wo + (size_t)(hkv * 256 + 32 * rb) * 1152 + cg * 576;
#pragma unroll 1
      for (int j = w4; j < 18; j += 4) { const u32 c = 64 * j + lane; const u32 row = (c * 1821u) >> 16; glds16<W_NT>(base, row * 576 + c * 16, lb + L_WO + j * 1024); }
    }
    {
      const char* base = (const char*)p.w2 + (size_t)(36 * u) * 1152 + cg * 576;
#pragma unroll 1
      for (int j = w4; j < 21; j += 4) { const u32 c = 64 * j + lane; const u32 row = (c * 1821u) >> 16; if (c < 1296) glds16<W_NT>(base, row * 576 + c * 16, lb + L_W2 + j * 1024); }
    }
  }
  u32x4 gx0[2], gx1[2], ggw[2];
  if (w == 8) {
    const GAS u32x4* xp = (const GAS u32x4*)p.x; const GAS u32x4* gp = (const GAS u32x4*)p.gw;
    gx0[0] = xp[lane]; gx1[0] = xp[72 + lane]; ggw[0] = gp[lane];
    const int l2 = 64 + (lane & 7);
    gx0[1] = xp[l2]; gx1[1] = xp[72 + l2]; ggw[1] = gp[l2];
  }
  const __amdgpu_buffer_rsrc_t rk = __builtin_amdgcn_make_buffer_rsrc((void*)p.K, 0, 0x10000000, 0x00020000);
  const __amdgpu_buffer_rsrc_t rv = __builtin_amdgcn_make_buffer_rsrc((void*)p.V, 0, 0x10000000, 0x00020000);
  const u32 kvoff = (u32)(((u32)(b * HKV + hkv) * SK + (SK - WIN) + s * 512 + w * 32) * (DH * 2));
  u32 tag;
  u64 tpub = 0;
  if (w < 4) {
    asm volatile("s_waitcnt vmcnt(0)" ::: "memory");
    tag = __builtin_amdgcn_readfirstlane(Eraw) + 1u;
    if (lane == 0) lds_set(L, L_FLAG + 4 + w * 4, tag);
    lds_wait4(L, L_FLAG + 4, tag);
    STAMP(2);
    const int cb = ii & 3, kq = ii >> 2;
    const u32 aoff = L_XA + (lane & 1) * 1152 + (144 * kq + 4 * g4) * 2;
    const u32 boff = L_WQ + (4 * g4 + q4) * 128 + p4 * 8 + w * 32;
    s16x4 fa[9], fb[9];
#pragma unroll
    for (int st = 0; st < 9; ++st) { fa[st] = lds_b64(L, aoff + st * 32); fb[st] = lds_tr(L, boff + st * 2048); }
    f32x4 acc = {0.f, 0.f, 0.f, 0.f};
#pragma unroll
    for (int st = 0; st < 9; ++st) acc = mfma16(fa[st], fb[st], acc);
    if (lane < 32) {
      const u32 o0 = WS_QP + (((hkv * 2 + g4) * 4 + kq) * 256 + 64 * cb + 16 * w + col) * 8;
      __builtin_amdgcn_raw_buffer_store_b64(u32x2{__float_as_uint(g4 ? acc[1] : acc[0]), tag}, rws, o0, 0, AUX_SC1);
    }
    tpub = __builtin_amdgcn_s_memrealtime();
    STAMP(3);
  }
  u32x4 kk[8], vv[8];
#pragma unroll
  for (int j = 0; j < 8; ++j) kk[j] = __builtin_amdgcn_raw_buffer_load_b128(rk, lane * 16, kvoff + j * 1024, AUX_NT);
#pragma unroll
  for (int j = 0; j < 8; ++j) vv[j] = __builtin_amdgcn_raw_buffer_load_b128(rv, lane * 16, kvoff + j * 1024, AUX_NT);
  __builtin_amdgcn_sched_barrier(0);
  STAMP(1);
  if (w >= 4) tag = __builtin_amdgcn_readfirstlane(Eraw) + 1u;
  if (w < 4) {
    const u32 go = WS_QP + (((hkv * 2 + b) * 4 + (lane >> 5)) * 256 + 64 * w + 2 * (lane & 31)) * 8;
    while (__builtin_amdgcn_s_memrealtime() - tpub < QDELAY) __builtin_amdgcn_s_sleep(2);
    u32x4 qa, qb;
#pragma unroll 1
    for (u32 spin = 0;; ++spin) {
      asm volatile("" ::: "memory");
      qa = __builtin_amdgcn_raw_buffer_load_b128(rws, go, 0, AUX_SC1);
      qb = __builtin_amdgcn_raw_buffer_load_b128(rws, go + 2 * 2048, 0, AUX_SC1);
      if (__all((qa[1] == tag) & (qa[3] == tag) & (qb[1] == tag) & (qb[3] == tag)) || spin > SPIN_CAP) break;
      __builtin_amdgcn_s_sleep(8);
    }
    const float c0 = x32_sum(__uint_as_float(qa[0]) + __uint_as_float(qb[0])), c1 = x32_sum(__uint_as_float(qa[2]) + __uint_as_float(qb[2]));
    if (lane < 32) *(LAS u32*)(L + L_QS + w * 128 + lane * 4) = cvtpk(c0, c1);
    asm volatile("s_waitcnt lgkmcnt(0)" ::: "memory");
    if (lane == 0) lds_set(L, L_FLAG + 36 + w * 4, tag);
    STAMP(4);
  }
  if (w >= 4 && w < 8) {
    asm volatile("s_waitcnt vmcnt(16)" ::: "memory");
    if (lane == 0) lds_set(L, L_FLAG + 20 + (w - 4) * 4, tag);
    if (wg < 216) {
      lds_wait4(L, L_FLAG + 20, tag);
      const int cb = wg / 3, kt = wg - 3 * cb;
      const u32 aoff = L_XB + (lane & 1) * 1152 + (192 * kt + 4 * g4) * 2;
      const u32 boff = L_W1 + (4 * g4 + q4) * 128 + p4 * 8 + (w - 4) * 32;
      f32x4 acc = {0.f, 0.f, 0.f, 0.f};
#pragma unroll
      for (int hf = 0; hf < 2; ++hf) {
        s16x4 fa[6], fb[6];
#pragma unroll
        for (int st = 0; st < 6; ++st) { fa[st] = lds_b64(L, aoff + (hf * 6 + st) * 32); fb[st] = lds_tr(L, boff + (hf * 6 + st) * 2048); }
#pragma unroll
        for (int st = 0; st < 6; ++st) acc = mfma16(fa[st], fb[st], acc);
      }
      if (lane < 32) {
        const u32 o0 = WS_HP + ((kt * 2 + g4) * F + 64 * cb + 16 * (w - 4) + col) * 8;
        __builtin_amdgcn_raw_buffer_store_b64(u32x2{__float_as_uint(g4 ? acc[1] : acc[0]), tag}, rws, o0, 0, AUX_SC1);
      }
    }
  } else if (w == 8) {
    float s0 = 0.f, s1 = 0.f;
#pragma unroll
    for (int r = 0; r < 2; ++r) {
      float t0 = 0.f, t1 = 0.f;
#pragma unroll
      for (int j = 0; j < 4; ++j) {
        const float glo = bf2f(ggw[r][j] & 0xffffu), ghi = __uint_as_float(ggw[r][j] & 0xffff0000u);
        t0 += rbf(bf2f(gx0[r][j] & 0xffffu) * glo) + rbf(__uint_as_float(gx0[r][j] & 0xffff0000u) * ghi);
        t1 += rbf(bf2f(gx1[r][j] & 0xffffu) * glo) + rbf(__uint_as_float(gx1[r][j] & 0xffff0000u) * ghi);
      }
      if (r == 0 || lane < 8) { s0 += t0; s1 += t1; }
    }
    s0 += dpp<XOR1>(s0); s0 += dpp<XOR2>(s0); s0 += dpp<ROR4>(s0); s0 += dpp<ROR8>(s0); s0 = x32_sum(x16_sum(s0));
    s1 += dpp<XOR1>(s1); s1 += dpp<XOR2>(s1); s1 += dpp<ROR4>(s1); s1 += dpp<ROR8>(s1); s1 = x32_sum(x16_sum(s1));
    const float g0 = rbf(1.f / (1.f + __expf(-rbf(s0)))), g1 = rbf(1.f / (1.f + __expf(-rbf(s1))));
    if (lane == 0) *(LAS f32x4*)(L + L_G) = f32x4{g0, g1, rbf(1.f - g0), rbf(1.f - g1)};
  }

  lds_wait4(L, L_FLAG + 36, tag);
  const u32x4 q0 = *(LAS u32x4*)(L + L_QS + col * 16), q1 = *(LAS u32x4*)(L + L_QS + 256 + col * 16);
  STAMP(5);
  float x[16];
#pragma unroll
  for (int i = 0; i < 8; ++i) {
    float d0 = 0.f, d1 = 0.f;
#pragma unroll
    for (int j = 0; j < 4; ++j) { d0 = dot2(kk[i][j], q0[j], d0); d1 = dot2(kk[i][j], q1[j], d1); }
    x[2 * i] = d0; x[2 * i + 1] = d1;
  }
#define TR_STEP(HALF, CTRL, BIT) _Pragma("unroll") for (int h = 0; h < (HALF); ++h) { \
    const float lo_ = x[h], hi_ = x[h + (HALF)]; const float t1_ = lo_ + dpp<CTRL>(lo_), t2_ = hi_ + dpp<CTRL>(hi_); \
    x[h] = (col & (BIT)) ? t2_ : t1_; }
  TR_STEP(8, XOR8, 8) TR_STEP(4, XOR7, 4) TR_STEP(2, XOR2, 2) TR_STEP(1, XOR1, 1)
#undef TR_STEP
  const float sc = rbf(rbf(x[0]) * 0.08838834764831845f) * LOG2E;
  float mx = sc;
  mx = fmaxf(mx, dpp<XOR2>(mx)); mx = fmaxf(mx, dpp<ROR4>(mx)); mx = fmaxf(mx, dpp<ROR8>(mx)); mx = x32_max(x16_max(mx));
  const float pr = __builtin_amdgcn_exp2f(sc - mx);
#define SEND_HINT1() { u32 old = 0; \
    if (lane == 0) old = __hip_atomic_fetch_add((LAS u32*)(L + L_FLAG), 1u, __ATOMIC_RELAXED, __HIP_MEMORY_SCOPE_WORKGROUP); \
    if (__builtin_amdgcn_readfirstlane(old) == 15u && lane < 16) __builtin_amdgcn_raw_buffer_store_b32(tag, rws, WS_F1 + ((hkv * 16 + lane) * 32 + ii) * 4, 0, AUX_SC1); }
  STAMP(6);
  f32x2 a0[4], a1[4];
#pragma unroll
  for (int j = 0; j < 4; ++j) { a0[j] = f32x2{0.f, 0.f}; a1[j] = f32x2{0.f, 0.f}; }
#define PV(i) { const float p0_ = dpp<BCAST + 2 * (i)>(pr), p1_ = dpp<BCAST + 2 * (i) + 1>(pr); \
    _Pragma("unroll") for (int j = 0; j < 4; ++j) { const f32x2 vf = {__uint_as_float(vv[i][j] << 16), __uint_as_float(vv[i][j] & 0xffff0000u)}; \
      a0[j] = __builtin_elementwise_fma(vf, f32x2{p0_, p0_}, a0[j]); a1[j] = __builtin_elementwise_fma(vf, f32x2{p1_, p1_}, a1[j]); } }
  float ls = pr;
  ls += dpp<XOR2>(ls); ls += dpp<ROR4>(ls); ls += dpp<ROR8>(ls); ls = x32_sum(x16_sum(ls));
  if (lane < 2) { *(LAS float*)(L + L_MM + lane * 64 + w * 4) = mx; *(LAS float*)(L + L_ML + lane * 64 + w * 4) = ls; }
  PV(0) PV(1) PV(2) PV(3) PV(4) PV(5) PV(6) PV(7)
#undef PV
  float y[8];
#pragma unroll
  for (int k = 0; k < 8; ++k) {
    auto r = __builtin_amdgcn_permlane32_swap(__float_as_uint(a0[k >> 1][k & 1]), __float_as_uint(a1[k >> 1][k & 1]), false, false);
    y[k] = __uint_as_float(r[0]) + __uint_as_float(r[1]);
  }
  f32x4 z;
#pragma unroll
  for (int k = 0; k < 4; ++k) {
    auto r = __builtin_amdgcn_permlane16_swap(__float_as_uint(y[k]), __float_as_uint(y[k + 4]), false, false);
    z[k] = __uint_as_float(r[0]) + __uint_as_float(r[1]);
  }
  *(LAS f32x4*)(L + L_MRG + w * 1024 + ((g4 >> 1) * 128 + 8 * col + 4 * (g4 & 1)) * 4) = z;
  SEND_HINT1()
  STAMP(7);
  if (w == 5) {
    const int cc = lane < 36 ? lane : 35;
    const u32 ho = WS_HP + (36 * u + cc) * 8;
    u32x2 hg[6];
    const float bb = bf2f(p.b1[36 * u + cc]);
    for (u32 spin = 0;; ++spin) {
      asm volatile("" ::: "memory");
#pragma unroll
      for (int j = 0; j < 6; ++j) hg[j] = __builtin_amdgcn_raw_buffer_load_b64(rws, ho + j * F * 8, 0, AUX_SC1);
      bool ok = true;
#pragma unroll
      for (int j = 0; j < 6; ++j) ok &= hg[j][1] == tag;
      if (__all(ok) || spin > SPIN_CAP) break;
      __builtin_amdgcn_s_sleep(2);
    }
#pragma unroll
    for (int bb2 = 0; bb2 < 2; ++bb2) {
      float hs = __uint_as_float(hg[bb2][0]) + __uint_as_float(hg[2 + bb2][0]) + __uint_as_float(hg[4 + bb2][0]);
      hs = rbf(rbf(hs) + bb);
      hs = hs > 0.f ? hs : 0.f;
      const u32 hv = lane < 36 ? (cvtpk(hs * hs, 0.f) & 0xffffu) : 0u;
      if (lane < 48) *(LAS u16*)(L + L_AH + bb2 * 96 + lane * 2) = (u16)hv;
    }
  }
  float bias = 0.f;
  const int fb_ = wg / 72, c72 = wg - 72 * fb_, fcg = c72 / 36, ch = c72 - 36 * fcg;
  if (w == 0 && wg < 144 && lane < 8) bias = bf2f(p.b2[fcg * 288 + ch * 8 + lane]);
  __syncthreads();
  STAMP(8);
  if (w < 4) {
    const int h = w >> 1, d = tid & 127;
    float ov[16];
#pragma unroll
    for (int j = 0; j < 16; ++j) ov[j] = *(LAS float*)(L + L_MRG + j * 1024 + tid * 4);
    const float mj = *(LAS float*)(L + L_MM + h * 64 + col * 4), lj = *(LAS float*)(L + L_ML + h * 64 + col * 4);
    float M = mj;
    M = fmaxf(M, dpp<XOR1>(M)); M = fmaxf(M, dpp<XOR2>(M)); M = fmaxf(M, dpp<ROR4>(M)); M = fmaxf(M, dpp<ROR8>(M));
    const float e = __builtin_amdgcn_exp2f(mj - M);
    float Ls = e * lj;
    Ls += dpp<XOR1>(Ls); Ls += dpp<XOR2>(Ls); Ls += dpp<ROR4>(Ls); Ls += dpp<ROR8>(Ls);
    float o = 0.f;
#pragma unroll
    for (int j = 0; j < 16; ++j) o = fmaf(__uint_as_float(__builtin_amdgcn_readlane(__float_as_uint(e), j)), ov[j], o);
    const u32 rec = WS_P1 + (((hkv * 8 + h * 4 + (d >> 5)) * 16) + b * 8 + s) * 288;
    __builtin_amdgcn_raw_buffer_store_b64(u32x2{__float_as_uint(o), tag}, rws, rec + (d & 31) * 8, 0, AUX_SC1);
    if ((d & 31) < 2) __builtin_amdgcn_raw_buffer_store_b64(u32x2{__float_as_uint((d & 1) ? Ls : M), tag}, rws, rec + 256 + (d & 1) * 8, 0, AUX_SC1);
    STAMP(9);
  } else if (w == 4) {
    const int gb = lane >> 5, gs = (lane >> 2) & 7, dq = lane & 3;
    const u32 rec = WS_P1 + (((hkv * 8 + rb) * 16) + gb * 8 + gs) * 288;
    u32x4 og[4], ml;
    wait_hint(rws, WS_F1 + ((hkv * 16 + ii) * 32 + (lane & 15)) * 4, tag);
    if (rb == 0) {
      __builtin_amdgcn_raw_buffer_store_b32(tag, rws, WS_F2 + (((lane & 1) * 72 + cg * 36 + (lane >> 1)) * 32 + hkv) * 4, 0, AUX_SC1);
      if (lane < 8) __builtin_amdgcn_raw_buffer_store_b32(tag, rws, WS_F2 + (((lane & 1) * 72 + cg * 36 + 32 + (lane >> 1)) * 32 + hkv) * 4, 0, AUX_SC1);
    }
    STAMP(15);
    if (HDELAY1) { const u64 t_ = __builtin_amdgcn_s_memrealtime(); while (__builtin_amdgcn_s_memrealtime() - t_ < HDELAY1) {} }
    for (u32 spin = 0;; ++spin) {
      asm volatile("" ::: "memory");
#pragma unroll
      for (int j = 0; j < 4; ++j) og[j] = __builtin_amdgcn_raw_buffer_load_b128(rws, rec + dq * 64 + j * 16, 0, AUX_SC1);
      ml = __builtin_amdgcn_raw_buffer_load_b128(rws, rec + 256, 0, AUX_SC1);
      bool ok = (ml[1] == tag) & (ml[3] == tag);
#pragma unroll
      for (int j = 0; j < 4; ++j) ok &= (og[j][1] == tag) & (og[j][3] == tag);
      if (__all(ok) || spin > SPIN_CAP) break;
      __builtin_amdgcn_s_sleep(RSLEEP);
    }
    const float m = __uint_as_float(ml[0]);
    float M = m;
    M = fmaxf(M, dpp<ROR4>(M)); M = fmaxf(M, dpp<ROR8>(M)); M = x16_max(M);
    const float e = __builtin_amdgcn_exp2f(m - M);
    float Ls = e * __uint_as_float(ml[2]);
    Ls += dpp<ROR4>(Ls); Ls += dpp<ROR8>(Ls); Ls = x16_sum(Ls);
    const float inv = 1.f / Ls;
    float ov[8];
#pragma unroll
    for (int k = 0; k < 8; ++k) {
      float t = e * __uint_as_float(og[k >> 1][(k & 1) * 2]);
      t += dpp<ROR4>(t); t += dpp<ROR8>(t); t = x16_sum(t);
      ov[k] = t * inv;
    }
    if (gs == 0) *(LAS u32x4*)(L + L_AO + gb * 64 + dq * 16) = u32x4{cvtpk(ov[0], ov[1]), cvtpk(ov[2], ov[3]), cvtpk(ov[4], ov[5]), cvtpk(ov[6], ov[7])};
    STAMP(10);
  }
  __syncthreads();
  STAMP(11);
  if (w < 9) {
    const u32 ao = L_AO + (lane & 1) * 64 + 4 * g4 * 2, ah = L_AH + (lane & 1) * 96 + 4 * g4 * 2;
    f32x4 da[2], df[2];
#pragma unroll
    for (int t = 0; t < 2; ++t) { da[t] = f32x4{0.f, 0.f, 0.f, 0.f}; df[t] = f32x4{0.f, 0.f, 0.f, 0.f}; }
    const int rr = 4 * g4 + q4;
#pragma unroll
    for (int st = 0; st < 2; ++st) {
      const s16x4 a = lds_b64(L, ao + st * 32);
#pragma unroll
      for (int t = 0; t < 2; ++t) da[t] = mfma16(a, lds_tr(L, L_WO + (16 * st + rr) * 576 + (2 * w + t) * 32 + p4 * 8), da[t]);
    }
#pragma unroll
    for (int st = 0; st < 3; ++st) {
      const s16x4 a = lds_b64(L, ah + st * 32);
      const int row = min(16 * st + rr, 35);
#pragma unroll
      for (int t = 0; t < 2; ++t) df[t] = mfma16(a, lds_tr(L, L_W2 + row * 576 + (2 * w + t) * 32 + p4 * 8), df[t]);
    }
    const int tb = g4 & 1, tt = g4 >> 1;
    const f32x4 gt = *(const LAS f32x4*)(L + L_G);
    const float va = tt ? (tb ? da[1][1] : da[1][0]) : (tb ? da[0][1] : da[0][0]);
    const float vf = tt ? (tb ? df[1][1] : df[1][0]) : (tb ? df[0][1] : df[0][0]);
    const float val = (tb ? gt[1] : gt[0]) * va + (tb ? gt[3] : gt[2]) * vf;
    const int n = (2 * w + tt) * 16 + col;
    const u32 off = WS_P2 + ((((cg * 2 + tb) * 36 + (n >> 3)) * 128) + u) * 64 + (n & 7) * 8;
    __builtin_amdgcn_raw_buffer_store_b64(u32x2{__float_as_uint(val), tag}, rws, off, 0, AUX_SC1);
    STAMP(12);
  }
  if (wg >= 144 || w >= 8) { STAMP_DUMP(); return; }
  {
    const u32 off = WS_P2 + (((fcg * 2 + fb_) * 36 + ch) * 128) * 64 + w * 1024 + lane * 16;
    u32x4 pg;
    wait_hint(rws, WS_F2 + (wg * 32 + (lane & 15)) * 4, tag);
    STAMP(14);
    if (HDELAY2) { const u64 t_ = __builtin_amdgcn_s_memrealtime(); while (__builtin_amdgcn_s_memrealtime() - t_ < HDELAY2) {} }
    for (u32 spin = 0;; ++spin) {
      asm volatile("" ::: "memory");
      pg = __builtin_amdgcn_raw_buffer_load_b128(rws, off, 0, AUX_SC1);
      if (__all((pg[1] == tag) & (pg[3] == tag)) || spin > SPIN_CAP) break;
      __builtin_amdgcn_s_sleep(RSLEEP);
    }
    float t0 = __uint_as_float(pg[0]), t1 = __uint_as_float(pg[2]);
    t0 += dpp<ROR4>(t0); t0 += dpp<ROR8>(t0); t0 = x32_sum(x16_sum(t0));
    t1 += dpp<ROR4>(t1); t1 += dpp<ROR8>(t1); t1 = x32_sum(x16_sum(t1));
    if (lane < 4) *(LAS f32x2*)(L + L_FIN + w * 32 + lane * 8) = f32x2{t0, t1};
    __syncthreads();
    if (w == 0) {
      if (lane < 8) {
        float r = 0.f;
#pragma unroll
        for (int j = 0; j < 8; ++j) r += *(LAS float*)(L + L_FIN + j * 32 + lane * 4);
        r += ((const LAS float*)(L + L_G))[2 + fb_] * bias;
        if (wg == 0 && lane == 0) { const u32 tot_ = (u32)(__builtin_amdgcn_s_memrealtime() - t00_); if (tot_ > 2200u) r += (float)tot_ * 0.001f; }
        p.out[fb_ * D + fcg * 288 + ch * 8 + lane] = (u16)(cvtpk(r, r) & 0xffffu);
      }
      if (wg == 0 && lane == 0) __hip_atomic_store((GAS u32*)(p.ws + WS_E), tag, __ATOMIC_RELAXED, __HIP_MEMORY_SCOPE_AGENT);
      STAMP(13);
    }
    STAMP_DUMP();
  }
}

extern "C" void kernel_launch(void* const* d_in, const int* in_sizes, int n_in, void* d_out, int out_size, void* d_ws, size_t ws_size,
                              hipStream_t stream) {
  Params p;
  p.x = (const u16*)d_in[0]; p.K = (const u16*)d_in[1]; p.V = (const u16*)d_in[2]; p.wq = (const u16*)d_in[3]; p.wo = (const u16*)d_in[4];
  p.w1 = (const u16*)d_in[6]; p.b1 = (const u16*)d_in[7]; p.w2 = (const u16*)d_in[8]; p.b2 = (const u16*)d_in[9]; p.gw = (const u16*)d_in[10];
  p.out = (u16*)d_out; p.ws = (unsigned char*)d_ws;
  (void)hipFuncSetAttribute((const void*)fused_kernel, hipFuncAttributeMaxDynamicSharedMemorySize, L_END);
  hipLaunchKernelGGL(fused_kernel, dim3(NWG), dim3(NTHR), L_END, stream, p);
}
```

```cpp
#include <hip/hip_runtime.h>
#include <stdint.h>

typedef unsigned short u16;
typedef unsigned int u32;
typedef unsigned long long u64;
typedef __attribute__((ext_vector_type(4))) unsigned u32x4;
typedef __attribute__((ext_vector_type(2))) unsigned u32x2;
typedef __attribute__((ext_vector_type(4))) float f32x4;
typedef __attribute__((ext_vector_type(2))) float f32x2;
typedef __attribute__((ext_vector_type(4))) short s16x4;
typedef __attribute__((ext_vector_type(2))) __bf16 bf16x2;
#define GAS __attribute__((address_space(1)))
#define LAS __attribute__((address_space(3)))

constexpr int D = 576, HKV = 16, SK = 32768, DH = 128, WIN = 4096, F = 4608;
constexpr int NWG = 256, NTHR = 1024;

constexpr u32 WS_E = 0;
constexpr u32 WS_QP = 256;
constexpr u32 WS_HP = WS_QP + 16 * 2 * 4 * 256 * 8;
constexpr u32 WS_P1 = WS_HP + 3 * 2 * F * 8;
constexpr u32 WS_P2 = WS_P1 + 16 * 8 * 16 * 288;
constexpr u32 WS_F1 = WS_P2 + 2 * 2 * 36 * 128 * 64;
constexpr u32 WS_F2 = WS_F1 + 16 * 16 * 128;
constexpr u32 WS_END = WS_F2 + 144 * 128;
constexpr u32 WS_STAMP = 64u << 20;

constexpr u32 L_XA = 0, L_WQ = 3072, L_W1 = L_WQ + 18432, L_WO = L_W1 + 24576, L_W2 = L_WO + 18432;
constexpr u32 L_MRG = L_W2 + 21504;
constexpr u32 L_MM = L_MRG + 16384;
constexpr u32 L_ML = L_MM + 128;
constexpr u32 L_QS = L_ML + 128;
constexpr u32 L_AO = L_QS + 512;
constexpr u32 L_AH = L_AO + 128;
constexpr u32 L_G = L_AH + 192;
constexpr u32 L_FLAG = L_G + 16;
constexpr u32 L_FIN = L_FLAG + 64;
constexpr u32 L_END = L_FIN + 256;
constexpr u32 FL_WQ = L_FLAG + 16, FL_W1 = L_FLAG + 32, FL_Q = L_FLAG + 48;

struct Params {
  const u16* x; const u16* K; const u16* V; const u16* wq; const u16* wo; const u16* w1;
  const u16* b1; const u16* w2; const u16* b2; const u16* gw; u16* out; unsigned char* ws;
};

constexpr int AUX_NT = 2, AUX_SC1 = 16;
#define W_NT 1
#define MAP 0
#define LATE_W 3
#define PIPE 1
#define WAIT_OWN 1
#define XSHIFT 2
#define QDELAY 220
#define HINT1_POS 3
#define HDELAY1 0
#define HDELAY2 0
#define RSLEEP 4
constexpr u32 SPIN_CAP = 1u << 16;
constexpr float LOG2E = 1.4426950408889634f;

__device__ __forceinline__ float bf2f(u32 v16) { return __uint_as_float(v16 << 16); }
__device__ __forceinline__ u32 cvtpk(float lo, float hi) {
  u32 r; asm("v_cvt_pk_bf16_f32 %0, %1, %2" : "=v"(r) : "v"(lo), "v"(hi)); return r;
}
__device__ __forceinline__ float rbf(float f) { return __uint_as_float(cvtpk(f, f) << 16); }
__device__ __forceinline__ float dot2(u32 a, u32 b, float acc) {
  return __builtin_amdgcn_fdot2_f32_bf16(__builtin_bit_cast(bf16x2, a), __builtin_bit_cast(bf16x2, b), acc, false);
}
template <int CTRL> __device__ __forceinline__ float dpp(float x) {
  return __builtin_bit_cast(float, __builtin_amdgcn_mov_dpp(__builtin_bit_cast(int, x), CTRL, 0xf, 0xf, true));
}
constexpr int XOR1 = 0xB1, XOR2 = 0x4E, XOR7 = 0x141, XOR8 = 0x128, ROR4 = 0x124, ROR8 = 0x128, BCAST = 0x150;
__device__ __forceinline__ float x16_max(float x) {
  auto s = __builtin_amdgcn_permlane16_swap(__float_as_uint(x), __float_as_uint(x), false, false);
  return fmaxf(__uint_as_float(s[0]), __uint_as_float(s[1]));
}
__device__ __forceinline__ float x32_max(float x) {
  auto s = __builtin_amdgcn_permlane32_swap(__float_as_uint(x), __float_as_uint(x), false, false);
  return fmaxf(__uint_as_float(s[0]), __uint_as_float(s[1]));
}
__device__ __forceinline__ float x16_sum(float x) {
  auto s = __builtin_amdgcn_permlane16_swap(__float_as_uint(x), __float_as_uint(x), false, false);
  return __uint_as_float(s[0]) + __uint_as_float(s[1]);
}
__device__ __forceinline__ float x32_sum(float x) {
  auto s = __builtin_amdgcn_permlane32_swap(__float_as_uint(x), __float_as_uint(x), false, false);
  return __uint_as_float(s[0]) + __uint_as_float(s[1]);
}
template <int NT> __device__ __forceinline__ void glds16(const void* sbase, u32 voff, u32 lds_dst) {
  unsigned keep;
  if (NT)
    asm volatile("s_mov_b32 %0, m0\n\ts_mov_b32 m0, %3\n\ts_nop 0\n\tglobal_load_lds_dwordx4 %1, %2 nt\n\ts_mov_b32 m0, %0" : "=&s"(keep) : "v"(voff), "s"(sbase), "s"(lds_dst) : "memory");
  else
    asm volatile("s_mov_b32 %0, m0\n\ts_mov_b32 m0, %3\n\ts_nop 0\n\tglobal_load_lds_dwordx4 %1, %2\n\ts_mov_b32 m0, %0" : "=&s"(keep) : "v"(voff), "s"(sbase), "s"(lds_dst) : "memory");
}
__device__ __forceinline__ f32x4 mfma16(s16x4 a, s16x4 b, f32x4 c) { return __builtin_amdgcn_mfma_f32_16x16x16bf16_1k(a, b, c, 0, 0, 0); }
__device__ __forceinline__ s16x4 lds_tr(LAS unsigned char* L, u32 off) { return __builtin_amdgcn_ds_read_tr16_b64_v4i16((LAS s16x4*)(L + off)); }
__device__ __forceinline__ s16x4 lds_b64(LAS unsigned char* L, u32 off) { return *(LAS s16x4*)(L + off); }
__device__ __forceinline__ u32 lds_flag(LAS unsigned char* L, u32 off) { return __hip_atomic_load((LAS u32*)(L + off), __ATOMIC_RELAXED, __HIP_MEMORY_SCOPE_WORKGROUP); }
__device__ __forceinline__ void lds_set(LAS unsigned char* L, u32 off, u32 v) { __hip_atomic_store((LAS u32*)(L + off), v, __ATOMIC_RELAXED, __HIP_MEMORY_SCOPE_WORKGROUP); }
__device__ __forceinline__ void lds_wait4(LAS unsigned char* L, u32 off) {
#pragma unroll 1
  for (u32 spin = 0; spin < (1u << 22); ++spin) {
    const u32 f = lds_flag(L, off + (__lane_id() & 3) * 4);
    if (__all(f != 0u)) break;
    __builtin_amdgcn_s_sleep(1);
  }
  asm volatile("" ::: "memory");
}
#define KEEP_POLLS(f) asm volatile("" :: "v"((f)[0]), "v"((f)[1]), "v"((f)[2]), "v"((f)[3]))
__device__ __forceinline__ void wait_hint(const unsigned char* ws, u32 off, u32 tag, u32 (&f)[4]) {
  u32 cnt;
  asm volatile(
      "global_load_dword %0, %5, %6 sc1\n\ts_sleep 2\n\t"
      "global_load_dword %1, %5, %6 sc1\n\ts_sleep 2\n\t"
      "global_load_dword %2, %5, %6 sc1\n\ts_sleep 2\n\t"
      "global_load_dword %3, %5, %6 sc1\n\t"
      "s_mov_b32 %4, 0x10000\n"
      "1:\n\t"
      "s_waitcnt vmcnt(3)\n\tv_cmp_ne_u32 vcc, %7, %0\n\ts_cbranch_vccz 2f\n\tglobal_load_dword %0, %5, %6 sc1\n\t"
      "s_waitcnt vmcnt(3)\n\tv_cmp_ne_u32 vcc, %7, %1\n\ts_cbranch_vccz 2f\n\tglobal_load_dword %1, %5, %6 sc1\n\t"
      "s_waitcnt vmcnt(3)\n\tv_cmp_ne_u32 vcc, %7, %2\n\ts_cbranch_vccz 2f\n\tglobal_load_dword %2, %5, %6 sc1\n\t"
      "s_waitcnt vmcnt(3)\n\tv_cmp_ne_u32 vcc, %7, %3\n\ts_cbranch_vccz 2f\n\tglobal_load_dword %3, %5, %6 sc1\n\t"
      "s_sub_u32 %4, %4, 1\n\ts_cmp_lg_u32 %4, 0\n\ts_cbranch_scc1 1b\n"
      "2:"
      : "=&v"(f[0]), "=&v"(f[1]), "=&v"(f[2]), "=&v"(f[3]), "=&s"(cnt)
      : "v"(off), "s"(ws), "s"(tag)
      : "vcc", "scc", "memory");
}
__device__ __forceinline__ void delay_ticks(u64 t0, u32 n) { while (__builtin_amdgcn_s_memrealtime() - t0 < n) __builtin_amdgcn_s_sleep(1); }

#define STAMP_DUMP() do {} while (0)
#define STAMP(i) do {} while (0)

template <int WHICH> __device__ __forceinline__ void load_wo_w2(const Params& p, u32 lb, int lane, int j0, int nw, int hkv, int rb, int cg, int u) {
  if (WHICH & 1) {
  const char* bo = (const char*)p.wo + (size_t)(hkv * 256 + 32 * rb) * 1152 + cg * 576;
#pragma unroll 1
  for (int j = j0; j < 18; j += nw) { const u32 c = 64 * j + lane; const u32 row = (c * 1821u) >> 16; glds16<W_NT>(bo, row * 576 + c * 16, lb + L_WO + j * 1024); }
  }
  if (WHICH & 2) {
  const char* b2 = (const char*)p.w2 + (size_t)(36 * u) * 1152 + cg * 576;
#pragma unroll 1
  for (int j = j0; j < 21; j += nw) { const u32 c = 64 * j + lane; const u32 row = (c * 1821u) >> 16; if (c < 1296) glds16<W_NT>(b2, row * 576 + c * 16, lb + L_W2 + j * 1024); }
  }
}

__global__ void __launch_bounds__(NTHR, 4) fused_kernel(Params p) {
  extern __shared__ __attribute__((aligned(16))) unsigned char smem[];
  LAS unsigned char* L = (LAS unsigned char*)smem;
  const u32 lb = (u32)(uintptr_t)L;
  const int tid = threadIdx.x, lane = tid & 63, w = __builtin_amdgcn_readfirstlane(tid >> 6);
  const int wg = blockIdx.x;
  const int xcd = wg & 7, idx = wg >> 3, hkv = xcd * 2 + (idx >> 4), ii = idx & 15, b = ii >> 3, s = ii & 7;
  const int hc = (hkv + XSHIFT) & 15;
  const int rb = ii >> 1, cg = ii & 1, u = hc * 8 + rb;
  const __amdgpu_buffer_rsrc_t rws = __builtin_amdgcn_make_buffer_rsrc((void*)p.ws, 0, WS_END, 0x00020000);
  STAMP(0);
  const int g4 = lane >> 4, col = lane & 15, q4 = col >> 2, p4 = lane & 3;
  if (w < 4) {
    const int cb = ii & 3, kq = ii >> 2;
    const u32 voff = (lane >> 3) * 8192 + (lane & 7) * 16;
    const char* base = (const char*)p.wq + (size_t)(144 * kq + 8 * w) * 8192 + (hkv * 256 + 64 * cb) * 2;
    if (w < 2) glds16<0>(p.x, w * 1024 + lane * 16, lb + L_XA + w * 1024);
    else if (w == 2) { if (lane < 16) glds16<0>(p.x, 2048 + lane * 16, lb + L_XA + 2048); }
#pragma unroll 1
    for (int j = w; j < 18; j += 4) { glds16<W_NT>(base, voff, lb + L_WQ + j * 1024); base += 32 * 8192; }
  } else if (w == 15) {
    if (lane < 16) lds_set(L, L_FLAG + lane * 4, 0u);
    asm volatile("s_waitcnt lgkmcnt(0)" ::: "memory");
  }
  __builtin_amdgcn_s_barrier();
  u32 Eraw;
  asm volatile("s_load_dword %0, %1, 0x0" : "=s"(Eraw) : "s"(p.ws) : "memory");
  if (w >= 4 && w < 8) {
    const int w4 = w - 4;
    if (wg < 216) {
      const int cb = wg / 3, kt = wg - 3 * cb;
      const u32 voff = (lane >> 3) * 9216 + (lane & 7) * 16;
      const char* base = (const char*)p.w1 + (size_t)(192 * kt + 8 * w4) * 9216 + cb * 128;
#pragma unroll 1
      for (int j = w4; j < 24; j += 4) { glds16<W_NT>(base, voff, lb + L_W1 + j * 1024); base += 32 * 9216; }
    }
    load_wo_w2<3 & ~LATE_W>(p, lb, lane, w4, 4, hc, rb, cg, u);
  }
  u32x4 ggw[2];
  if (w == 8) {
    const GAS u32x4* gp = (const GAS u32x4*)p.gw;
    ggw[0] = gp[lane]; ggw[1] = gp[64 + (lane & 7)];
  }
  const __amdgpu_buffer_rsrc_t rk = __builtin_amdgcn_make_buffer_rsrc((void*)p.K, 0, 0x10000000, 0x00020000);
  const __amdgpu_buffer_rsrc_t rv = __builtin_amdgcn_make_buffer_rsrc((void*)p.V, 0, 0x10000000, 0x00020000);
  const u32 kvoff = (u32)(((u32)(b * HKV + hkv) * SK + (SK - WIN) + s * 512 + w * 32) * (DH * 2));
  u64 tpub = 0;
  if (w < 4) {
    asm volatile("s_waitcnt vmcnt(0) lgkmcnt(0)" : "+s"(Eraw) :: "memory");
    const u32 tag = Eraw + 1u;
    if (lane == 0) lds_set(L, FL_WQ + w * 4, 1u);
    lds_wait4(L, FL_WQ);
    STAMP(2);
    const int cb = ii & 3, kq = ii >> 2;
    const u32 aoff = L_XA + (lane & 1) * 1152 + (144 * kq + 4 * g4) * 2;
    const u32 boff = L_WQ + (4 * g4 + q4) * 128 + p4 * 8 + w * 32;
    s16x4 fa[9], fb[9];
#pragma unroll
    for (int st = 0; st < 9; ++st) { fa[st] = lds_b64(L, aoff + st * 32); fb[st] = lds_tr(L, boff + st * 2048); }
    f32x4 acc = {0.f, 0.f, 0.f, 0.f};
#pragma unroll
    for (int st = 0; st < 9; ++st) acc = mfma16(fa[st], fb[st], acc);
    if (lane < 32) {
      const u32 o0 = WS_QP + (((hkv * 2 + g4) * 4 + kq) * 256 + 64 * cb + 16 * w + col) * 8;
      __builtin_amdgcn_raw_buffer_store_b64(u32x2{__float_as_uint(g4 ? acc[1] : acc[0]), tag}, rws, o0, 0, AUX_SC1);
    }
    tpub = __builtin_amdgcn_s_memrealtime();
    STAMP(3);
  }
  u32x4 kk[8], vv[8];
#pragma unroll
  for (int j = 0; j < 8; ++j) kk[j] = __builtin_amdgcn_raw_buffer_load_b128(rk, lane * 16, kvoff + j * 1024, AUX_NT);
#pragma unroll
  for (int j = 0; j < 8; ++j) vv[j] = __builtin_amdgcn_raw_buffer_load_b128(rv, lane * 16, kvoff + j * 1024, AUX_NT);
  __builtin_amdgcn_sched_barrier(0);
  if (lane == 0) __hip_atomic_fetch_add((LAS u32*)(L + L_FLAG + 4), 1u, __ATOMIC_RELAXED, __HIP_MEMORY_SCOPE_WORKGROUP);
  STAMP(1);
  asm volatile("s_waitcnt lgkmcnt(0)" : "+s"(Eraw) :: "memory");
  const u32 tag = Eraw + 1u;
  if (w < 4) {
    const u32 go = WS_QP + (((hkv * 2 + b) * 4 + (lane >> 5)) * 256 + 64 * w + 2 * (lane & 31)) * 8;
    delay_ticks(tpub, QDELAY);
    u32x4 qa, qb;
#pragma unroll 1
    for (u32 spin = 0;; ++spin) {
      asm volatile("" ::: "memory");
      qa = __builtin_amdgcn_raw_buffer_load_b128(rws, go, 0, AUX_SC1);
      qb = __builtin_amdgcn_raw_buffer_load_b128(rws, go + 2 * 2048, 0, AUX_SC1);
      if (__all((qa[1] == tag) & (qa[3] == tag) & (qb[1] == tag) & (qb[3] == tag)) || spin > SPIN_CAP) break;
      __builtin_amdgcn_s_sleep(8);
    }
    const float c0 = x32_sum(__uint_as_float(qa[0]) + __uint_as_float(qb[0])), c1 = x32_sum(__uint_as_float(qa[2]) + __uint_as_float(qb[2]));
    if (lane < 32) *(LAS u32*)(L + L_QS + w * 128 + lane * 4) = cvtpk(c0, c1);
    asm volatile("s_waitcnt lgkmcnt(0)" ::: "memory");
    if (lane == 0) lds_set(L, FL_Q + w * 4, 1u);
    STAMP(4);
  } else if (w < 8) {
    asm volatile("s_waitcnt vmcnt(16)" ::: "memory");
    if (lane == 0) lds_set(L, FL_W1 + (w - 4) * 4, 1u);
    if (wg < 216) {
      lds_wait4(L, FL_WQ);
      lds_wait4(L, FL_W1);
      const int cb = wg / 3, kt = wg - 3 * cb;
      const u32 aoff = L_XA + (lane & 1) * 1152 + (192 * kt + 4 * g4) * 2;
      const u32 boff = L_W1 + (4 * g4 + q4) * 128 + p4 * 8 + (w - 4) * 32;
      f32x4 acc = {0.f, 0.f, 0.f, 0.f};
#pragma unroll
      for (int hf = 0; hf < 2; ++hf) {
        s16x4 fa[6], fb[6];
#pragma unroll
        for (int st = 0; st < 6; ++st) { fa[st] = lds_b64(L, aoff + (hf * 6 + st) * 32); fb[st] = lds_tr(L, boff + (hf * 6 + st) * 2048); }
#pragma unroll
        for (int st = 0; st < 6; ++st) acc = mfma16(fa[st], fb[st], acc);
      }
      if (lane < 32) {
        const u32 o0 = WS_HP + ((kt * 2 + g4) * F + 64 * cb + 16 * (w - 4) + col) * 8;
        __builtin_amdgcn_raw_buffer_store_b64(u32x2{__float_as_uint(g4 ? acc[1] : acc[0]), tag}, rws, o0, 0, AUX_SC1);
      }
    }
  } else if (w == 8) {
    lds_wait4(L, FL_WQ);
    float s0 = 0.f, s1 = 0.f;
#pragma unroll
    for (int r = 0; r < 2; ++r) {
      const u32 xo = L_XA + (r ? 64 + (lane & 7) : lane) * 16;
      const u32x4 x0 = *(LAS u32x4*)(L + xo), x1 = *(LAS u32x4*)(L + xo + 1152);
      float t0 = 0.f, t1 = 0.f;
#pragma unroll
      for (int j = 0; j < 4; ++j) {
        const float glo = bf2f(ggw[r][j] & 0xffffu), ghi = __uint_as_float(ggw[r][j] & 0xffff0000u);
        t0 += rbf(bf2f(x0[j] & 0xffffu) * glo) + rbf(__uint_as_float(x0[j] & 0xffff0000u) * ghi);
        t1 += rbf(bf2f(x1[j] & 0xffffu) * glo) + rbf(__uint_as_float(x1[j] & 0xffff0000u) * ghi);
      }
      if (r == 0 || lane < 8) { s0 += t0; s1 += t1; }
    }
    s0 += dpp<XOR1>(s0); s0 += dpp<XOR2>(s0); s0 += dpp<ROR4>(s0); s0 += dpp<ROR8>(s0); s0 = x32_sum(x16_sum(s0));
    s1 += dpp<XOR1>(s1); s1 += dpp<XOR2>(s1); s1 += dpp<ROR4>(s1); s1 += dpp<ROR8>(s1); s1 = x32_sum(x16_sum(s1));
    const float g0 = rbf(1.f / (1.f + __expf(-rbf(s0)))), g1 = rbf(1.f / (1.f + __expf(-rbf(s1))));
    if (lane == 0) *(LAS f32x4*)(L + L_G) = f32x4{g0, g1, rbf(1.f - g0), rbf(1.f - g1)};
  }

  lds_wait4(L, FL_Q);
  const u32x4 q0 = *(LAS u32x4*)(L + L_QS + col * 16), q1 = *(LAS u32x4*)(L + L_QS + 256 + col * 16);
  STAMP(5);
  float x[16];
#pragma unroll
  for (int i = 0; i < 8; ++i) {
    float d0 = 0.f, d1 = 0.f;
#pragma unroll
    for (int j = 0; j < 4; ++j) { d0 = dot2(kk[i][j], q0[j], d0); d1 = dot2(kk[i][j], q1[j], d1); }
    x[2 * i] = d0; x[2 * i + 1] = d1;
  }
#define TR_STEP(HALF, CTRL, BIT) _Pragma("unroll") for (int h = 0; h < (HALF); ++h) { \
    const float lo_ = x[h], hi_ = x[h + (HALF)]; const float t1_ = lo_ + dpp<CTRL>(lo_), t2_ = hi_ + dpp<CTRL>(hi_); \
    x[h] = (col & (BIT)) ? t2_ : t1_; }
  TR_STEP(8, XOR8, 8) TR_STEP(4, XOR7, 4) TR_STEP(2, XOR2, 2) TR_STEP(1, XOR1, 1)
#undef TR_STEP
  const float sc = rbf(rbf(x[0]) * 0.08838834764831845f) * LOG2E;
  float mx = sc;
  mx = fmaxf(mx, dpp<XOR2>(mx)); mx = fmaxf(mx, dpp<ROR4>(mx)); mx = fmaxf(mx, dpp<ROR8>(mx)); mx = x32_max(x16_max(mx));
  const float pr = __builtin_amdgcn_exp2f(sc - mx);
#define SEND_HINT1() { u32 old = 0; \
    if (lane == 0) old = __hip_atomic_fetch_add((LAS u32*)(L + L_FLAG), 1u, __ATOMIC_RELAXED, __HIP_MEMORY_SCOPE_WORKGROUP); \
    if (__builtin_amdgcn_readfirstlane(old) == 15u && lane < 16) __builtin_amdgcn_raw_buffer_store_b32(tag, rws, WS_F1 + ((hkv * 16 + lane) * 32 + ii) * 4, 0, AUX_SC1); }
  STAMP(6);
  f32x2 a0[4], a1[4];
#pragma unroll
  for (int j = 0; j < 4; ++j) { a0[j] = f32x2{0.f, 0.f}; a1[j] = f32x2{0.f, 0.f}; }
#define PV(i) { const float p0_ = dpp<BCAST + 2 * (i)>(pr), p1_ = dpp<BCAST + 2 * (i) + 1>(pr); \
    _Pragma("unroll") for (int j = 0; j < 4; ++j) { const f32x2 vf = {__uint_as_float(vv[i][j] << 16), __uint_as_float(vv[i][j] & 0xffff0000u)}; \
      a0[j] = __builtin_elementwise_fma(vf, f32x2{p0_, p0_}, a0[j]); a1[j] = __builtin_elementwise_fma(vf, f32x2{p1_, p1_}, a1[j]); } }
  float ls = pr;
  ls += dpp<XOR2>(ls); ls += dpp<ROR4>(ls); ls += dpp<ROR8>(ls); ls = x32_sum(x16_sum(ls));
  if (lane < 2) { *(LAS float*)(L + L_MM + lane * 64 + w * 4) = mx; *(LAS float*)(L + L_ML + lane * 64 + w * 4) = ls; }
  PV(0) PV(1) PV(2) PV(3) PV(4) PV(5) PV(6) PV(7)
#undef PV
  float y[8];
#pragma unroll
  for (int k = 0; k < 8; ++k) {
    auto r = __builtin_amdgcn_permlane32_swap(__float_as_uint(a0[k >> 1][k & 1]), __float_as_uint(a1[k >> 1][k & 1]), false, false);
    y[k] = __uint_as_float(r[0]) + __uint_as_float(r[1]);
  }
  f32x4 z;
#pragma unroll
  for (int k = 0; k < 4; ++k) {
    auto r = __builtin_amdgcn_permlane16_swap(__float_as_uint(y[k]), __float_as_uint(y[k + 4]), false, false);
    z[k] = __uint_as_float(r[0]) + __uint_as_float(r[1]);
  }
  *(LAS f32x4*)(L + L_MRG + w * 1024 + ((g4 >> 1) * 128 + 8 * col + 4 * (g4 & 1)) * 4) = z;
  asm volatile("s_waitcnt lgkmcnt(0)" ::: "memory");
  if (lane == 0) __hip_atomic_fetch_add((LAS u32*)(L + L_FLAG + 8), 1u, __ATOMIC_RELAXED, __HIP_MEMORY_SCOPE_WORKGROUP);
  STAMP(7);
  if (w == 5) {
    const int cc = lane < 36 ? lane : 35;
    const u32 ho = WS_HP + (36 * u + cc) * 8;
    u32x2 hg[6];
    const float bb = bf2f(p.b1[36 * u + cc]);
#pragma unroll 1
    for (u32 spin = 0;; ++spin) {
      asm volatile("" ::: "memory");
#pragma unroll
      for (int j = 0; j < 6; ++j) hg[j] = __builtin_amdgcn_raw_buffer_load_b64(rws, ho + j * F * 8, 0, AUX_SC1);
      bool ok = true;
#pragma unroll
      for (int j = 0; j < 6; ++j) ok &= hg[j][1] == tag;
      if (__all(ok) || spin > SPIN_CAP) break;
      __builtin_amdgcn_s_sleep(16);
    }
#pragma unroll
    for (int bb2 = 0; bb2 < 2; ++bb2) {
      float hs = __uint_as_float(hg[bb2][0]) + __uint_as_float(hg[2 + bb2][0]) + __uint_as_float(hg[4 + bb2][0]);
      hs = rbf(rbf(hs) + bb);
      hs = hs > 0.f ? hs : 0.f;
      const u32 hv = lane < 36 ? (cvtpk(hs * hs, 0.f) & 0xffffu) : 0u;
      if (lane < 48) *(LAS u16*)(L + L_AH + bb2 * 96 + lane * 2) = (u16)hv;
    }
  }
  float bias = 0.f;
  const int fb_ = wg / 72, c72 = wg - 72 * fb_, fcg = c72 / 36, ch = c72 - 36 * fcg;
  if (w == 0 && wg < 144 && lane < 8) bias = bf2f(p.b2[fcg * 288 + ch * 8 + lane]);
  if (w >= 5 && w < 9) {
#pragma unroll 1
    for (u32 spin = 0; spin < (1u << 22); ++spin) {
      if (lds_flag(L, L_FLAG + 4) == 16u) break;
      __builtin_amdgcn_s_sleep(1);
    }
    load_wo_w2<LATE_W>(p, lb, lane, w - 5, 4, hc, rb, cg, u);
    STAMP(3);
  }
  if (w < 4) {
#pragma unroll 1
    for (u32 spin = 0; spin < (1u << 22); ++spin) {
      if (lds_flag(L, L_FLAG + 8) == 16u) break;
    }
    asm volatile("" ::: "memory");
    if (lane < 4) __builtin_amdgcn_raw_buffer_store_b32(tag, rws, WS_F1 + ((hkv * 16 + w * 4 + lane) * 32 + ii) * 4, 0, AUX_SC1);
  }
  STAMP(8);
  if (w < 4) {
    const int h = w >> 1, d = tid & 127;
    float ov[16];
#pragma unroll
    for (int j = 0; j < 16; ++j) ov[j] = *(LAS float*)(L + L_MRG + j * 1024 + tid * 4);
    const float mj = *(LAS float*)(L + L_MM + h * 64 + col * 4), lj = *(LAS float*)(L + L_ML + h * 64 + col * 4);
    float M = mj;
    M = fmaxf(M, dpp<XOR1>(M)); M = fmaxf(M, dpp<XOR2>(M)); M = fmaxf(M, dpp<ROR4>(M)); M = fmaxf(M, dpp<ROR8>(M));
    const float e = __builtin_amdgcn_exp2f(mj - M);
    float Ls = e * lj;
    Ls += dpp<XOR1>(Ls); Ls += dpp<XOR2>(Ls); Ls += dpp<ROR4>(Ls); Ls += dpp<ROR8>(Ls);
    float o = 0.f;
#pragma unroll
    for (int j = 0; j < 16; ++j) o = fmaf(__uint_as_float(__builtin_amdgcn_readlane(__float_as_uint(e), j)), ov[j], o);
    const u32 rec = WS_P1 + (((hkv * 8 + h * 4 + (d >> 5)) * 16) + b * 8 + s) * 288;
    __builtin_amdgcn_raw_buffer_store_b64(u32x2{__float_as_uint(o), tag}, rws, rec + (d & 31) * 8, 0, AUX_SC1);
    if ((d & 31) < 2) __builtin_amdgcn_raw_buffer_store_b64(u32x2{__float_as_uint((d & 1) ? Ls : M), tag}, rws, rec + 256 + (d & 1) * 8, 0, AUX_SC1);
    STAMP(9);
  } else if (w == 4) {
    const int gb = lane >> 5, gs = (lane >> 2) & 7, dq = lane & 3;
    const u32 rec = WS_P1 + (((hc * 8 + rb) * 16) + gb * 8 + gs) * 288;
    u32x4 og[4], ml;
    u32 pf[4];
#pragma unroll 1
    for (u32 spin = 0; spin < (1u << 22); ++spin) { if (lds_flag(L, L_FLAG + 8) == 16u) break; __builtin_amdgcn_s_sleep(1); }
    wait_hint(p.ws, WS_F1 + ((hc * 16 + ii) * 32 + (lane & 15)) * 4, tag, pf);
    if (rb == 0) {
      __builtin_amdgcn_raw_buffer_store_b32(tag, rws, WS_F2 + (((lane & 1) * 72 + cg * 36 + (lane >> 1)) * 32 + hc) * 4, 0, AUX_SC1);
      if (lane < 8) __builtin_amdgcn_raw_buffer_store_b32(tag, rws, WS_F2 + (((lane & 1) * 72 + cg * 36 + 32 + (lane >> 1)) * 32 + hc) * 4, 0, AUX_SC1);
    }
    STAMP(15);
    if (HDELAY1) delay_ticks(__builtin_amdgcn_s_memrealtime(), HDELAY1);
#pragma unroll 1
    for (u32 spin = 0;; ++spin) {
      asm volatile("" ::: "memory");
#pragma unroll
      for (int j = 0; j < 4; ++j) og[j] = __builtin_amdgcn_raw_buffer_load_b128(rws, rec + dq * 64 + j * 16, 0, AUX_SC1);
      ml = __builtin_amdgcn_raw_buffer_load_b128(rws, rec + 256, 0, AUX_SC1);
      bool ok = (ml[1] == tag) & (ml[3] == tag);
#pragma unroll
      for (int j = 0; j < 4; ++j) ok &= (og[j][1] == tag) & (og[j][3] == tag);
      if (__all(ok) || spin > SPIN_CAP) break;
      __builtin_amdgcn_s_sleep(RSLEEP);
    }
    KEEP_POLLS(pf);
    const float m = __uint_as_float(ml[0]);
    float M = m;
    M = fmaxf(M, dpp<ROR4>(M)); M = fmaxf(M, dpp<ROR8>(M)); M = x16_max(M);
    const float e = __builtin_amdgcn_exp2f(m - M);
    float Ls = e * __uint_as_float(ml[2]);
    Ls += dpp<ROR4>(Ls); Ls += dpp<ROR8>(Ls); Ls = x16_sum(Ls);
    const float inv = 1.f / Ls;
    float ov[8];
#pragma unroll
    for (int k = 0; k < 8; ++k) {
      float t = e * __uint_as_float(og[k >> 1][(k & 1) * 2]);
      t += dpp<ROR4>(t); t += dpp<ROR8>(t); t = x16_sum(t);
      ov[k] = t * inv;
    }
    if (gs == 0) *(LAS u32x4*)(L + L_AO + gb * 64 + dq * 16) = u32x4{cvtpk(ov[0], ov[1]), cvtpk(ov[2], ov[3]), cvtpk(ov[4], ov[5]), cvtpk(ov[6], ov[7])};
    STAMP(10);
  }
  else if (w < 9) { asm volatile("s_waitcnt vmcnt(0)" ::: "memory"); STAMP(10); }
  asm volatile("s_waitcnt lgkmcnt(0)\n\ts_barrier" ::: "memory");
  STAMP(11);
  if (w < 9) {
    const u32 ao = L_AO + (lane & 1) * 64 + 4 * g4 * 2, ah = L_AH + (lane & 1) * 96 + 4 * g4 * 2;
    f32x4 da[2], df[2];
#pragma unroll
    for (int t = 0; t < 2; ++t) { da[t] = f32x4{0.f, 0.f, 0.f, 0.f}; df[t] = f32x4{0.f, 0.f, 0.f, 0.f}; }
    const int rr = 4 * g4 + q4;
#pragma unroll
    for (int st = 0; st < 2; ++st) {
      const s16x4 a = lds_b64(L, ao + st * 32);
#pragma unroll
      for (int t = 0; t < 2; ++t) da[t] = mfma16(a, lds_tr(L, L_WO + (16 * st + rr) * 576 + (2 * w + t) * 32 + p4 * 8), da[t]);
    }
#pragma unroll
    for (int st = 0; st < 3; ++st) {
      const s16x4 a = lds_b64(L, ah + st * 32);
      const int row = min(16 * st + rr, 35);
#pragma unroll
      for (int t = 0; t < 2; ++t) df[t] = mfma16(a, lds_tr(L, L_W2 + row * 576 + (2 * w + t) * 32 + p4 * 8), df[t]);
    }
    const int tb = g4 & 1, tt = g4 >> 1;
    const f32x4 gt = *(const LAS f32x4*)(L + L_G);
    const float va = tt ? (tb ? da[1][1] : da[1][0]) : (tb ? da[0][1] : da[0][0]);
    const float vf = tt ? (tb ? df[1][1] : df[1][0]) : (tb ? df[0][1] : df[0][0]);
    const float val = (tb ? gt[1] : gt[0]) * va + (tb ? gt[3] : gt[2]) * vf;
    const int n = (2 * w + tt) * 16 + col;
    const u32 off = WS_P2 + ((((cg * 2 + tb) * 36 + (n >> 3)) * 128) + u) * 64 + (n & 7) * 8;
    __builtin_amdgcn_raw_buffer_store_b64(u32x2{__float_as_uint(val), tag}, rws, off, 0, AUX_SC1);
    STAMP(12);
  }
  if (wg >= 144 || w >= 8) { STAMP_DUMP(); return; }
  {
    const u32 off = WS_P2 + (((fcg * 2 + fb_) * 36 + ch) * 128) * 64 + w * 1024 + lane * 16;
    u32x4 pg;
    u32 pf[4];
    wait_hint(p.ws, WS_F2 + (wg * 32 + (lane & 15)) * 4, tag, pf);
    STAMP(14);
    if (HDELAY2) delay_ticks(__builtin_amdgcn_s_memrealtime(), HDELAY2);
#pragma unroll 1
    for (u32 spin = 0;; ++spin) {
      asm volatile("" ::: "memory");
      pg = __builtin_amdgcn_raw_buffer_load_b128(rws, off, 0, AUX_SC1);
      if (__all((pg[1] == tag) & (pg[3] == tag)) || spin > SPIN_CAP) break;
      __builtin_amdgcn_s_sleep(RSLEEP);
    }
    KEEP_POLLS(pf);
    float t0 = __uint_as_float(pg[0]), t1 = __uint_as_float(pg[2]);
    t0 += dpp<ROR4>(t0); t0 += dpp<ROR8>(t0); t0 = x32_sum(x16_sum(t0));
    t1 += dpp<ROR4>(t1); t1 += dpp<ROR8>(t1); t1 = x32_sum(x16_sum(t1));
    if (lane < 4) *(LAS f32x2*)(L + L_FIN + w * 32 + lane * 8) = f32x2{t0, t1};
    __syncthreads();
    if (w == 0) {
      if (lane < 8) {
        float r = 0.f;
#pragma unroll
        for (int j = 0; j < 8; ++j) r += *(LAS float*)(L + L_FIN + j * 32 + lane * 4);
        r += ((const LAS float*)(L + L_G))[2 + fb_] * bias;
        p.out[fb_ * D + fcg * 288 + ch * 8 + lane] = (u16)(cvtpk(r, r) & 0xffffu);
      }
      if (wg == 0 && lane == 0) __hip_atomic_store((GAS u32*)(p.ws + WS_E), tag, __ATOMIC_RELAXED, __HIP_MEMORY_SCOPE_AGENT);
      STAMP(13);
    }
    STAMP_DUMP();
  }
}

extern "C" void kernel_launch(void* const* d_in, const int* in_sizes, int n_in, void* d_out, int out_size, void* d_ws, size_t ws_size,
                              hipStream_t stream) {
  Params p;
  p.x = (const u16*)d_in[0]; p.K = (const u16*)d_in[1]; p.V = (const u16*)d_in[2]; p.wq = (const u16*)d_in[3]; p.wo = (const u16*)d_in[4];
  p.w1 = (const u16*)d_in[6]; p.b1 = (const u16*)d_in[7]; p.w2 = (const u16*)d_in[8]; p.b2 = (const u16*)d_in[9]; p.gw = (const u16*)d_in[10];
  p.out = (u16*)d_out; p.ws = (unsigned char*)d_ws;
  (void)hipFuncSetAttribute((const void*)fused_kernel, hipFuncAttributeMaxDynamicSharedMemorySize, L_END);
  hipLaunchKernelGGL(fused_kernel, dim3(NWG), dim3(NTHR), L_END, stream, p);
}
```

```cpp
#include <hip/hip_runtime.h>
#include <stdint.h>

typedef unsigned short u16;
typedef unsigned int u32;
typedef unsigned long long u64;
typedef __attribute__((ext_vector_type(4))) unsigned u32x4;
typedef __attribute__((ext_vector_type(2))) unsigned u32x2;
typedef __attribute__((ext_vector_type(4))) float f32x4;
typedef __attribute__((ext_vector_type(2))) float f32x2;
typedef __attribute__((ext_vector_type(4))) short s16x4;
typedef __attribute__((ext_vector_type(2))) __bf16 bf16x2;
#define GAS __attribute__((address_space(1)))
#define LAS __attribute__((address_space(3)))

constexpr int D = 576, HKV = 16, SK = 32768, DH = 128, WIN = 4096, F = 4608;
constexpr int NWG = 256, NTHR = 1024;

constexpr u32 WS_E = 0;
constexpr u32 WS_QP = 256;
constexpr u32 WS_HP = WS_QP + 16 * 2 * 4 * 256 * 8;
constexpr u32 WS_P1 = WS_HP + 3 * 2 * F * 8;
constexpr u32 WS_P2 = WS_P1 + 16 * 8 * 16 * 288;
constexpr u32 WS_F1 = WS_P2 + 2 * 2 * 36 * 128 * 64;
constexpr u32 WS_F2 = WS_F1 + 16 * 16 * 128;
constexpr u32 WS_END = WS_F2 + 144 * 128;
constexpr u32 WS_STAMP = 64u << 20;

constexpr u32 L_XA = 0, L_WQ = 3072, L_W1 = L_WQ + 18432, L_WO = L_W1 + 24576, L_W2 = L_WO + 18432;
constexpr u32 L_MRG = L_W2 + 21504;
constexpr u32 L_MM = L_MRG + 16384;
constexpr u32 L_ML = L_MM + 128;
constexpr u32 L_QS = L_ML + 128;
constexpr u32 L_AO = L_QS + 512;
constexpr u32 L_AH = L_AO + 128;
constexpr u32 L_G = L_AH + 192;
constexpr u32 L_FLAG = L_G + 16;
constexpr u32 L_FIN = L_FLAG + 64;
constexpr u32 L_END = L_FIN + 256;
constexpr u32 FL_WQ = L_FLAG + 16, FL_W1 = L_FLAG + 32, FL_Q = L_FLAG + 48;

struct Params {
  const u16* x; const u16* K; const u16* V; const u16* wq; const u16* wo; const u16* w1;
  const u16* b1; const u16* w2; const u16* b2; const u16* gw; u16* out; unsigned char* ws;
};

constexpr int AUX_NT = 2, AUX_SC1 = 16;
#define W_NT 1
#define MAP 0
#define LATE_W 3
#define PIPE 1
#define WAIT_OWN 1
#define XSHIFT 2
#define QDELAY 220
#define HINT1_POS 3
#define HDELAY1 0
#define HDELAY2 0
#define RSLEEP 4
constexpr u32 SPIN_CAP = 1u << 16;
constexpr float LOG2E = 1.4426950408889634f;

__device__ __forceinline__ float bf2f(u32 v16) { return __uint_as_float(v16 << 16); }
__device__ __forceinline__ u32 cvtpk(float lo, float hi) {
  u32 r; asm("v_cvt_pk_bf16_f32 %0, %1, %2" : "=v"(r) : "v"(lo), "v"(hi)); return r;
}
__device__ __forceinline__ float rbf(float f) { return __uint_as_float(cvtpk(f, f) << 16); }
__device__ __forceinline__ float dot2(u32 a, u32 b, float acc) {
  return __builtin_amdgcn_fdot2_f32_bf16(__builtin_bit_cast(bf16x2, a), __builtin_bit_cast(bf16x2, b), acc, false);
}
template <int CTRL> __device__ __forceinline__ float dpp(float x) {
  return __builtin_bit_cast(float, __builtin_amdgcn_mov_dpp(__builtin_bit_cast(int, x), CTRL, 0xf, 0xf, true));
}
constexpr int XOR1 = 0xB1, XOR2 = 0x4E, XOR7 = 0x141, XOR8 = 0x128, ROR4 = 0x124, ROR8 = 0x128, BCAST = 0x150;
__device__ __forceinline__ float x16_max(float x) {
  auto s = __builtin_amdgcn_permlane16_swap(__float_as_uint(x), __float_as_uint(x), false, false);
  return fmaxf(__uint_as_float(s[0]), __uint_as_float(s[1]));
}
__device__ __forceinline__ float x32_max(float x) {
  auto s = __builtin_amdgcn_permlane32_swap(__float_as_uint(x), __float_as_uint(x), false, false);
  return fmaxf(__uint_as_float(s[0]), __uint_as_float(s[1]));
}
__device__ __forceinline__ float x16_sum(float x) {
  auto s = __builtin_amdgcn_permlane16_swap(__float_as_uint(x), __float_as_uint(x), false, false);
  return __uint_as_float(s[0]) + __uint_as_float(s[1]);
}
__device__ __forceinline__ float x32_sum(float x) {
  auto s = __builtin_amdgcn_permlane32_swap(__float_as_uint(x), __float_as_uint(x), false, false);
  return __uint_as_float(s[0]) + __uint_as_float(s[1]);
}
template <int NT> __device__ __forceinline__ void glds16(const void* sbase, u32 voff, u32 lds_dst) {
  unsigned keep;
  if (NT)
    asm volatile("s_mov_b32 %0, m0\n\ts_mov_b32 m0, %3\n\ts_nop 0\n\tglobal_load_lds_dwordx4 %1, %2 nt\n\ts_mov_b32 m0, %0" : "=&s"(keep) : "v"(voff), "s"(sbase), "s"(lds_dst) : "memory");
  else
    asm volatile("s_mov_b32 %0, m0\n\ts_mov_b32 m0, %3\n\ts_nop 0\n\tglobal_load_lds_dwordx4 %1, %2\n\ts_mov_b32 m0, %0" : "=&s"(keep) : "v"(voff), "s"(sbase), "s"(lds_dst) : "memory");
}
__device__ __forceinline__ f32x4 mfma16(s16x4 a, s16x4 b, f32x4 c) { return __builtin_amdgcn_mfma_f32_16x16x16bf16_1k(a, b, c, 0, 0, 0); }
__device__ __forceinline__ s16x4 lds_tr(LAS unsigned char* L, u32 off) { return __builtin_amdgcn_ds_read_tr16_b64_v4i16((LAS s16x4*)(L + off)); }
__device__ __forceinline__ s16x4 lds_b64(LAS unsigned char* L, u32 off) { return *(LAS s16x4*)(L + off); }
__device__ __forceinline__ u32 lds_flag(LAS unsigned char* L, u32 off) { return __hip_atomic_load((LAS u32*)(L + off), __ATOMIC_RELAXED, __HIP_MEMORY_SCOPE_WORKGROUP); }
__device__ __forceinline__ void lds_set(LAS unsigned char* L, u32 off, u32 v) { __hip_atomic_store((LAS u32*)(L + off), v, __ATOMIC_RELAXED, __HIP_MEMORY_SCOPE_WORKGROUP); }
__device__ __forceinline__ void lds_wait4(LAS unsigned char* L, u32 off) {
#pragma unroll 1
  for (u32 spin = 0; spin < (1u << 22); ++spin) {
    const u32 f = lds_flag(L, off + (__lane_id() & 3) * 4);
    if (__all(f != 0u)) break;
    __builtin_amdgcn_s_sleep(1);
  }
  asm volatile("" ::: "memory");
}
#define KEEP_POLLS(f) asm volatile("" :: "v"((f)[0]), "v"((f)[1]), "v"((f)[2]), "v"((f)[3]))
__device__ __forceinline__ void wait_hint(const unsigned char* ws, u32 off, u32 tag, u32 (&f)[4]) {
  u32 cnt;
  asm volatile(
      "global_load_dword %0, %5, %6 sc1\n\ts_sleep 2\n\t"
      "global_load_dword %1, %5, %6 sc1\n\ts_sleep 2\n\t"
      "global_load_dword %2, %5, %6 sc1\n\ts_sleep 2\n\t"
      "global_load_dword %3, %5, %6 sc1\n\t"
      "s_mov_b32 %4, 0x10000\n"
      "1:\n\t"
      "s_waitcnt vmcnt(3)\n\tv_cmp_ne_u32 vcc, %7, %0\n\ts_cbranch_vccz 2f\n\tglobal_load_dword %0, %5, %6 sc1\n\t"
      "s_waitcnt vmcnt(3)\n\tv_cmp_ne_u32 vcc, %7, %1\n\ts_cbranch_vccz 2f\n\tglobal_load_dword %1, %5, %6 sc1\n\t"
      "s_waitcnt vmcnt(3)\n\tv_cmp_ne_u32 vcc, %7, %2\n\ts_cbranch_vccz 2f\n\tglobal_load_dword %2, %5, %6 sc1\n\t"
      "s_waitcnt vmcnt(3)\n\tv_cmp_ne_u32 vcc, %7, %3\n\ts_cbranch_vccz 2f\n\tglobal_load_dword %3, %5, %6 sc1\n\t"
      "s_sub_u32 %4, %4, 1\n\ts_cmp_lg_u32 %4, 0\n\ts_cbranch_scc1 1b\n"
      "2:"
      : "=&v"(f[0]), "=&v"(f[1]), "=&v"(f[2]), "=&v"(f[3]), "=&s"(cnt)
      : "v"(off), "s"(ws), "s"(tag)
      : "vcc", "scc", "memory");
}
__device__ __forceinline__ void delay_ticks(u64 t0, u32 n) { while (__builtin_amdgcn_s_memrealtime() - t0 < n) __builtin_amdgcn_s_sleep(1); }

#define STAMP_DUMP() do {} while (0)
#define STAMP(i) do {} while (0)

template <int WHICH> __device__ __forceinline__ void load_wo_w2(const Params& p, u32 lb, int lane, int j0, int nw, int hkv, int rb, int cg, int u) {
  if (WHICH & 1) {
  const char* bo = (const char*)p.wo + (size_t)(hkv * 256 + 32 * rb) * 1152 + cg * 576;
#pragma unroll 1
  for (int j = j0; j < 18; j += nw) { const u32 c = 64 * j + lane; const u32 row = (c * 1821u) >> 16; glds16<W_NT>(bo, row * 576 + c * 16, lb + L_WO + j * 1024); }
  }
  if (WHICH & 2) {
  const char* b2 = (const char*)p.w2 + (size_t)(36 * u) * 1152 + cg * 576;
#pragma unroll 1
  for (int j = j0; j < 21; j += nw) { const u32 c = 64 * j + lane; const u32 row = (c * 1821u) >> 16; if (c < 1296) glds16<W_NT>(b2, row * 576 + c * 16, lb + L_W2 + j * 1024); }
  }
}

__global__ void __launch_bounds__(NTHR, 4) fused_kernel(Params p) {
  extern __shared__ __attribute__((aligned(16))) unsigned char smem[];
  LAS unsigned char* L = (LAS unsigned char*)smem;
  const u32 lb = (u32)(uintptr_t)L;
  const int tid = threadIdx.x, lane = tid & 63, w = __builtin_amdgcn_readfirstlane(tid >> 6);
  const int wg = blockIdx.x;
  const int xcd = wg & 7, idx = wg >> 3, hkv = xcd * 2 + (idx >> 4), ii = idx & 15, b = ii >> 3, s = ii & 7;
  const int hc = (hkv + XSHIFT) & 15;
  const int rb = ii >> 1, cg = ii & 1, u = hc * 8 + rb;
  const __amdgpu_buffer_rsrc_t rws = __builtin_amdgcn_make_buffer_rsrc((void*)p.ws, 0, WS_END, 0x00020000);
  STAMP(0);
  const int g4 = lane >> 4, col = lane & 15, q4 = col >> 2, p4 = lane & 3;
  if (w < 4) {
    const int cb = ii & 3, kq = ii >> 2;
    const u32 voff = (lane >> 3) * 8192 + (lane & 7) * 16;
    const char* base = (const char*)p.wq + (size_t)(144 * kq + 8 * w) * 8192 + (hkv * 256 + 64 * cb) * 2;
    if (w < 2) glds16<0>(p.x, w * 1024 + lane * 16, lb + L_XA + w * 1024);
    else if (w == 2) { if (lane < 16) glds16<0>(p.x, 2048 + lane * 16, lb + L_XA + 2048); }
#pragma unroll 1
    for (int j = w; j < 18; j += 4) { glds16<W_NT>(base, voff, lb + L_WQ + j * 1024); base += 32 * 8192; }
  } else if (w == 15) {
    if (lane < 16) lds_set(L, L_FLAG + lane * 4, 0u);
    asm volatile("s_waitcnt lgkmcnt(0)" ::: "memory");
  }
  __builtin_amdgcn_s_barrier();
  u32 Eraw;
  asm volatile("s_load_dword %0, %1, 0x0" : "=s"(Eraw) : "s"(p.ws) : "memory");
  if (w >= 4 && w < 8) {
    const int w4 = w - 4;
    if (wg < 216) {
      const int cb = wg / 3, kt = wg - 3 * cb;
      const u32 voff = (lane >> 3) * 9216 + (lane & 7) * 16;
      const char* base = (const char*)p.w1 + (size_t)(192 * kt + 8 * w4) * 9216 + cb * 128;
#pragma unroll 1
      for (int j = w4; j < 24; j += 4) { glds16<W_NT>(base, voff, lb + L_W1 + j * 1024); base += 32 * 9216; }
    }
    load_wo_w2<3 & ~LATE_W>(p, lb, lane, w4, 4, hc, rb, cg, u);
  }
  u32x4 ggw[2];
  if (w == 8) {
    const GAS u32x4* gp = (const GAS u32x4*)p.gw;
    ggw[0] = gp[lane]; ggw[1] = gp[64 + (lane & 7)];
  }
  const __amdgpu_buffer_rsrc_t rk = __builtin_amdgcn_make_buffer_rsrc((void*)p.K, 0, 0x10000000, 0x00020000);
  const __amdgpu_buffer_rsrc_t rv = __builtin_amdgcn_make_buffer_rsrc((void*)p.V, 0, 0x10000000, 0x00020000);
  const u32 kvoff = (u32)(((u32)(b * HKV + hkv) * SK + (SK - WIN) + s * 512 + w * 32) * (DH * 2));
  u64 tpub = 0;
  if (w < 4) {
    asm volatile("s_waitcnt vmcnt(0) lgkmcnt(0)" : "+s"(Eraw) :: "memory");
    const u32 tag = Eraw + 1u;
    if (lane == 0) lds_set(L, FL_WQ + w * 4, 1u);
    lds_wait4(L, FL_WQ);
    STAMP(2);
    const int cb = ii & 3, kq = ii >> 2;
    const u32 aoff = L_XA + (lane & 1) * 1152 + (144 * kq + 4 * g4) * 2;
    const u32 boff = L_WQ + (4 * g4 + q4) * 128 + p4 * 8 + w * 32;
    s16x4 fa[9], fb[9];
#pragma unroll
    for (int st = 0; st < 9; ++st) { fa[st] = lds_b64(L, aoff + st * 32); fb[st] = lds_tr(L, boff + st * 2048); }
    f32x4 acc = {0.f, 0.f, 0.f, 0.f};
#pragma unroll
    for (int st = 0; st < 9; ++st) acc = mfma16(fa[st], fb[st], acc);
    if (lane < 32) {
      const u32 o0 = WS_QP + (((hkv * 2 + g4) * 4 + kq) * 256 + 64 * cb + 16 * w + col) * 8;
      __builtin_amdgcn_raw_buffer_store_b64(u32x2{__float_as_uint(g4 ? acc[1] : acc[0]), tag}, rws, o0, 0, AUX_SC1);
    }
    tpub = __builtin_amdgcn_s_memrealtime();
    STAMP(3);
  }
  u32x4 kk[8], vv[8];
#pragma unroll
  for (int j = 0; j < 8; ++j) kk[j] = __builtin_amdgcn_raw_buffer_load_b128(rk, lane * 16, kvoff + j * 1024, AUX_NT);
#pragma unroll
  for (int j = 0; j < 8; ++j) vv[j] = __builtin_amdgcn_raw_buffer_load_b128(rv, lane * 16, kvoff + j * 1024, AUX_NT);
  __builtin_amdgcn_sched_barrier(0);
  if (lane == 0) __hip_atomic_fetch_add((LAS u32*)(L + L_FLAG + 4), 1u, __ATOMIC_RELAXED, __HIP_MEMORY_SCOPE_WORKGROUP);
  STAMP(1);
  asm volatile("s_waitcnt lgkmcnt(0)" : "+s"(Eraw) :: "memory");
  const u32 tag = Eraw + 1u;
  if (w < 4) {
    const u32 go = WS_QP + (((hkv * 2 + b) * 4 + (lane >> 5)) * 256 + 64 * w + 2 * (lane & 31)) * 8;
    delay_ticks(tpub, QDELAY);
    u32x4 qa, qb;
#pragma unroll 1
    for (u32 spin = 0;; ++spin) {
      asm volatile("" ::: "memory");
      qa = __builtin_amdgcn_raw_buffer_load_b128(rws, go, 0, AUX_SC1);
      qb = __builtin_amdgcn_raw_buffer_load_b128(rws, go + 2 * 2048, 0, AUX_SC1);
      if (__all((qa[1] == tag) & (qa[3] == tag) & (qb[1] == tag) & (qb[3] == tag)) || spin > SPIN_CAP) break;
      __builtin_amdgcn_s_sleep(8);
    }
    const float c0 = x32_sum(__uint_as_float(qa[0]) + __uint_as_float(qb[0])), c1 = x32_sum(__uint_as_float(qa[2]) + __uint_as_float(qb[2]));
    if (lane < 32) *(LAS u32*)(L + L_QS + w * 128 + lane * 4) = cvtpk(c0, c1);
    asm volatile("s_waitcnt lgkmcnt(0)" ::: "memory");
    if (lane == 0) lds_set(L, FL_Q + w * 4, 1u);
    STAMP(4);
  } else if (w < 8) {
    asm volatile("s_waitcnt vmcnt(16)" ::: "memory");
    if (lane == 0) lds_set(L, FL_W1 + (w - 4) * 4, 1u);
    if (wg < 216) {
      lds_wait4(L, FL_WQ);
      lds_wait4(L, FL_W1);
      const int cb = wg / 3, kt = wg - 3 * cb;
      const u32 aoff = L_XA + (lane & 1) * 1152 + (192 * kt + 4 * g4) * 2;
      const u32 boff = L_W1 + (4 * g4 + q4) * 128 + p4 * 8 + (w - 4) * 32;
      f32x4 acc = {0.f, 0.f, 0.f, 0.f};
#pragma unroll
      for (int hf = 0; hf < 2; ++hf) {
        s16x4 fa[6], fb[6];
#pragma unroll
        for (int st = 0; st < 6; ++st) { fa[st] = lds_b64(L, aoff + (hf * 6 + st) * 32); fb[st] = lds_tr(L, boff + (hf * 6 + st) * 2048); }
#pragma unroll
        for (int st = 0; st < 6; ++st) acc = mfma16(fa[st], fb[st], acc);
      }
      if (lane < 32) {
        const u32 o0 = WS_HP + ((kt * 2 + g4) * F + 64 * cb + 16 * (w - 4) + col) * 8;
        __builtin_amdgcn_raw_buffer_store_b64(u32x2{__float_as_uint(g4 ? acc[1] : acc[0]), tag}, rws, o0, 0, AUX_SC1);
      }
    }
  } else if (w == 8) {
    lds_wait4(L, FL_WQ);
    float s0 = 0.f, s1 = 0.f;
#pragma unroll
    for (int r = 0; r < 2; ++r) {
      const u32 xo = L_XA + (r ? 64 + (lane & 7) : lane) * 16;
      const u32x4 x0 = *(LAS u32x4*)(L + xo), x1 = *(LAS u32x4*)(L + xo + 1152);
      float t0 = 0.f, t1 = 0.f;
#pragma unroll
      for (int j = 0; j < 4; ++j) {
        const float glo = bf2f(ggw[r][j] & 0xffffu), ghi = __uint_as_float(ggw[r][j] & 0xffff0000u);
        t0 += rbf(bf2f(x0[j] & 0xffffu) * glo) + rbf(__uint_as_float(x0[j] & 0xffff0000u) * ghi);
        t1 += rbf(bf2f(x1[j] & 0xffffu) * glo) + rbf(__uint_as_float(x1[j] & 0xffff0000u) * ghi);
      }
      if (r == 0 || lane < 8) { s0 += t0; s1 += t1; }
    }
    s0 += dpp<XOR1>(s0); s0 += dpp<XOR2>(s0); s0 += dpp<ROR4>(s0); s0 += dpp<ROR8>(s0); s0 = x32_sum(x16_sum(s0));
    s1 += dpp<XOR1>(s1); s1 += dpp<XOR2>(s1); s1 += dpp<ROR4>(s1); s1 += dpp<ROR8>(s1); s1 = x32_sum(x16_sum(s1));
    const float g0 = rbf(1.f / (1.f + __expf(-rbf(s0)))), g1 = rbf(1.f / (1.f + __expf(-rbf(s1))));
    if (lane == 0) *(LAS f32x4*)(L + L_G) = f32x4{g0, g1, rbf(1.f - g0), rbf(1.f - g1)};
  }

  lds_wait4(L, FL_Q);
  const u32x4 q0 = *(LAS u32x4*)(L + L_QS + col * 16), q1 = *(LAS u32x4*)(L + L_QS + 256 + col * 16);
  STAMP(5);
  float x[16];
#pragma unroll
  for (int i = 0; i < 8; ++i) {
    float d0 = 0.f, d1 = 0.f;
#pragma unroll
    for (int j = 0; j < 4; ++j) { d0 = dot2(kk[i][j], q0[j], d0); d1 = dot2(kk[i][j], q1[j], d1); }
    x[2 * i] = d0; x[2 * i + 1] = d1;
  }
#define TR_STEP(HALF, CTRL, BIT) _Pragma("unroll") for (int h = 0; h < (HALF); ++h) { \
    const float lo_ = x[h], hi_ = x[h + (HALF)]; const float t1_ = lo_ + dpp<CTRL>(lo_), t2_ = hi_ + dpp<CTRL>(hi_); \
    x[h] = (col & (BIT)) ? t2_ : t1_; }
  TR_STEP(8, XOR8, 8) TR_STEP(4, XOR7, 4) TR_STEP(2, XOR2, 2) TR_STEP(1, XOR1, 1)
#undef TR_STEP
  const float sc = rbf(rbf(x[0]) * 0.08838834764831845f) * LOG2E;
  float mx = sc;
  mx = fmaxf(mx, dpp<XOR2>(mx)); mx = fmaxf(mx, dpp<ROR4>(mx)); mx = fmaxf(mx, dpp<ROR8>(mx)); mx = x32_max(x16_max(mx));
  const float pr = __builtin_amdgcn_exp2f(sc - mx);
#define SEND_HINT1() { u32 old = 0; \
    if (lane == 0) old = __hip_atomic_fetch_add((LAS u32*)(L + L_FLAG), 1u, __ATOMIC_RELAXED, __HIP_MEMORY_SCOPE_WORKGROUP); \
    if (__builtin_amdgcn_readfirstlane(old) == 15u && lane < 16) __builtin_amdgcn_raw_buffer_store_b32(tag, rws, WS_F1 + ((hkv * 16 + lane) * 32 + ii) * 4, 0, AUX_SC1); }
  STAMP(6);
  f32x2 a0[4], a1[4];
#pragma unroll
  for (int j = 0; j < 4; ++j) { a0[j] = f32x2{0.f, 0.f}; a1[j] = f32x2{0.f, 0.f}; }
#define PV(i) { const float p0_ = dpp<BCAST + 2 * (i)>(pr), p1_ = dpp<BCAST + 2 * (i) + 1>(pr); \
    _Pragma("unroll") for (int j = 0; j < 4; ++j) { const f32x2 vf = {__uint_as_float(vv[i][j] << 16), __uint_as_float(vv[i][j] & 0xffff0000u)}; \
      a0[j] = __builtin_elementwise_fma(vf, f32x2{p0_, p0_}, a0[j]); a1[j] = __builtin_elementwise_fma(vf, f32x2{p1_, p1_}, a1[j]); } }
  float ls = pr;
  ls += dpp<XOR2>(ls); ls += dpp<ROR4>(ls); ls += dpp<ROR8>(ls); ls = x32_sum(x16_sum(ls));
  if (lane < 2) { *(LAS float*)(L + L_MM + lane * 64 + w * 4) = mx; *(LAS float*)(L + L_ML + lane * 64 + w * 4) = ls; }
  PV(0) PV(1) PV(2) PV(3) PV(4) PV(5) PV(6) PV(7)
#undef PV
  float y[8];
#pragma unroll
  for (int k = 0; k < 8; ++k) {
    auto r = __builtin_amdgcn_permlane32_swap(__float_as_uint(a0[k >> 1][k & 1]), __float_as_uint(a1[k >> 1][k & 1]), false, false);
    y[k] = __uint_as_float(r[0]) + __uint_as_float(r[1]);
  }
  f32x4 z;
#pragma unroll
  for (int k = 0; k < 4; ++k) {
    auto r = __builtin_amdgcn_permlane16_swap(__float_as_uint(y[k]), __float_as_uint(y[k + 4]), false, false);
    z[k] = __uint_as_float(r[0]) + __uint_as_float(r[1]);
  }
  *(LAS f32x4*)(L + L_MRG + w * 1024 + ((g4 >> 1) * 128 + 8 * col + 4 * (g4 & 1)) * 4) = z;
  asm volatile("s_waitcnt lgkmcnt(0)" ::: "memory");
  if (lane == 0) __hip_atomic_fetch_add((LAS u32*)(L + L_FLAG + 8), 1u, __ATOMIC_RELAXED, __HIP_MEMORY_SCOPE_WORKGROUP);
  STAMP(7);
  if (w == 5) {
    const int cc = lane < 36 ? lane : 35;
    const u32 ho = WS_HP + (36 * u + cc) * 8;
    u32x2 hg[6];
    const float bb = bf2f(p.b1[36 * u + cc]);
#pragma unroll 1
    for (u32 spin = 0;; ++spin) {
      asm volatile("" ::: "memory");
#pragma unroll
      for (int j = 0; j < 6; ++j) hg[j] = __builtin_amdgcn_raw_buffer_load_b64(rws, ho + j * F * 8, 0, AUX_SC1);
      bool ok = true;
#pragma unroll
      for (int j = 0; j < 6; ++j) ok &= hg[j][1] == tag;
      if (__all(ok) || spin > SPIN_CAP) break;
      __builtin_amdgcn_s_sleep(16);
    }
#pragma unroll
    for (int bb2 = 0; bb2 < 2; ++bb2) {
      float hs = __uint_as_float(hg[bb2][0]) + __uint_as_float(hg[2 + bb2][0]) + __uint_as_float(hg[4 + bb2][0]);
      hs = rbf(rbf(hs) + bb);
      hs = hs > 0.f ? hs : 0.f;
      const u32 hv = lane < 36 ? (cvtpk(hs * hs, 0.f) & 0xffffu) : 0u;
      if (lane < 48) *(LAS u16*)(L + L_AH + bb2 * 96 + lane * 2) = (u16)hv;
    }
  }
  float bias = 0.f;
  const int fb_ = wg / 72, c72 = wg - 72 * fb_, fcg = c72 / 36, ch = c72 - 36 * fcg;
  if (w == 0 && wg < 144 && lane < 8) bias = bf2f(p.b2[fcg * 288 + ch * 8 + lane]);
  if (w >= 5 && w < 9) {
#pragma unroll 1
    for (u32 spin = 0; spin < (1u << 22); ++spin) {
      if (lds_flag(L, L_FLAG + 4) == 16u) break;
      __builtin_amdgcn_s_sleep(1);
    }
    load_wo_w2<LATE_W>(p, lb, lane, w - 5, 4, hc, rb, cg, u);
    STAMP(3);
  }
  if (w < 4) {
#pragma unroll 1
    for (u32 spin = 0; spin < (1u << 22); ++spin) {
      if (lds_flag(L, L_FLAG + 8) == 16u) break;
    }
    asm volatile("" ::: "memory");
    if (lane < 4) __builtin_amdgcn_raw_buffer_store_b32(tag, rws, WS_F1 + ((hkv * 16 + w * 4 + lane) * 32 + ii) * 4, 0, AUX_SC1);
  }
  STAMP(8);
  if (w < 4) {
    const int h = w >> 1, d = tid & 127;
    float ov[16];
#pragma unroll
    for (int j = 0; j < 16; ++j) ov[j] = *(LAS float*)(L + L_MRG + j * 1024 + tid * 4);
    const float mj = *(LAS float*)(L + L_MM + h * 64 + col * 4), lj = *(LAS float*)(L + L_ML + h * 64 + col * 4);
    float M = mj;
    M = fmaxf(M, dpp<XOR1>(M)); M = fmaxf(M, dpp<XOR2>(M)); M = fmaxf(M, dpp<ROR4>(M)); M = fmaxf(M, dpp<ROR8>(M));
    const float e = __builtin_amdgcn_exp2f(mj - M);
    float Ls = e * lj;
    Ls += dpp<XOR1>(Ls); Ls += dpp<XOR2>(Ls); Ls += dpp<ROR4>(Ls); Ls += dpp<ROR8>(Ls);
    float o = 0.f;
#pragma unroll
    for (int j = 0; j < 16; ++j) o = fmaf(__uint_as_float(__builtin_amdgcn_readlane(__float_as_uint(e), j)), ov[j], o);
    const u32 rec = WS_P1 + (((hkv * 8 + h * 4 + (d >> 5)) * 16) + b * 8 + s) * 288;
    __builtin_amdgcn_raw_buffer_store_b64(u32x2{__float_as_uint(o), tag}, rws, rec + (d & 31) * 8, 0, AUX_SC1);
    if ((d & 31) < 2) __builtin_amdgcn_raw_buffer_store_b64(u32x2{__float_as_uint((d & 1) ? Ls : M), tag}, rws, rec + 256 + (d & 1) * 8, 0, AUX_SC1);
    STAMP(9);
  } else if (w == 4) {
    const int gb = lane >> 5, gs = (lane >> 2) & 7, dq = lane & 3;
    const u32 rec = WS_P1 + (((hc * 8 + rb) * 16) + gb * 8 + gs) * 288;
    u32x4 og[4], ml;
    u32 pf[4];
#pragma unroll 1
    for (u32 spin = 0; spin < (1u << 22); ++spin) { if (lds_flag(L, L_FLAG + 8) == 16u) break; __builtin_amdgcn_s_sleep(1); }
    wait_hint(p.ws, WS_F1 + ((hc * 16 + ii) * 32 + (lane & 15)) * 4, tag, pf);
    if (rb == 0) {
      __builtin_amdgcn_raw_buffer_store_b32(tag, rws, WS_F2 + (((lane & 1) * 72 + cg * 36 + (lane >> 1)) * 32 + hc) * 4, 0, AUX_SC1);
      if (lane < 8) __builtin_amdgcn_raw_buffer_store_b32(tag, rws, WS_F2 + (((lane & 1) * 72 + cg * 36 + 32 + (lane >> 1)) * 32 + hc) * 4, 0, AUX_SC1);
    }
    STAMP(15);
    if (HDELAY1) delay_ticks(__builtin_amdgcn_s_memrealtime(), HDELAY1);
#pragma unroll 1
    for (u32 spin = 0;; ++spin) {
      asm volatile("" ::: "memory");
#pragma unroll
      for (int j = 0; j < 4; ++j) og[j] = __builtin_amdgcn_raw_buffer_load_b128(rws, rec + dq * 64 + j * 16, 0, AUX_SC1);
      ml = __builtin_amdgcn_raw_buffer_load_b128(rws, rec + 256, 0, AUX_SC1);
      bool ok = (ml[1] == tag) & (ml[3] == tag);
#pragma unroll
      for (int j = 0; j < 4; ++j) ok &= (og[j][1] == tag) & (og[j][3] == tag);
      if (__all(ok) || spin > SPIN_CAP) break;
      __builtin_amdgcn_s_sleep(RSLEEP);
    }
    KEEP_POLLS(pf);
    const float m = __uint_as_float(ml[0]);
    float M = m;
    M = fmaxf(M, dpp<ROR4>(M)); M = fmaxf(M, dpp<ROR8>(M)); M = x16_max(M);
    const float e = __builtin_amdgcn_exp2f(m - M);
    float Ls = e * __uint_as_float(ml[2]);
    Ls += dpp<ROR4>(Ls); Ls += dpp<ROR8>(Ls); Ls = x16_sum(Ls);
    const float inv = 1.f / Ls;
    float ov[8];
#pragma unroll
    for (int k = 0; k < 8; ++k) {
      float t = e * __uint_as_float(og[k >> 1][(k & 1) * 2]);
      t += dpp<ROR4>(t); t += dpp<ROR8>(t); t = x16_sum(t);
      ov[k] = t * inv;
    }
    if (gs == 0) *(LAS u32x4*)(L + L_AO + gb * 64 + dq * 16) = u32x4{cvtpk(ov[0], ov[1]), cvtpk(ov[2], ov[3]), cvtpk(ov[4], ov[5]), cvtpk(ov[6], ov[7])};
    STAMP(10);
  }
  else if (w < 9) { asm volatile("s_waitcnt vmcnt(0)" ::: "memory"); STAMP(10); }
  asm volatile("s_waitcnt lgkmcnt(0)\n\ts_barrier" ::: "memory");
  STAMP(11);
  if (w < 9) {
    const u32 ao = L_AO + (lane & 1) * 64 + 4 * g4 * 2, ah = L_AH + (lane & 1) * 96 + 4 * g4 * 2;
    f32x4 da[2], df[2];
#pragma unroll
    for (int t = 0; t < 2; ++t) { da[t] = f32x4{0.f, 0.f, 0.f, 0.f}; df[t] = f32x4{0.f, 0.f, 0.f, 0.f}; }
    const int rr = 4 * g4 + q4;
#pragma unroll
    for (int st = 0; st < 2; ++st) {
      const s16x4 a = lds_b64(L, ao + st * 32);
#pragma unroll
      for (int t = 0; t < 2; ++t) da[t] = mfma16(a, lds_tr(L, L_WO + (16 * st + rr) * 576 + (2 * w + t) * 32 + p4 * 8), da[t]);
    }
#pragma unroll
    for (int st = 0; st < 3; ++st) {
      const s16x4 a = lds_b64(L, ah + st * 32);
      const int row = min(16 * st + rr, 35);
#pragma unroll
      for (int t = 0; t < 2; ++t) df[t] = mfma16(a, lds_tr(L, L_W2 + row * 576 + (2 * w + t) * 32 + p4 * 8), df[t]);
    }
    const int tb = g4 & 1, tt = g4 >> 1;
    const f32x4 gt = *(const LAS f32x4*)(L + L_G);
    const float va = tt ? (tb ? da[1][1] : da[1][0]) : (tb ? da[0][1] : da[0][0]);
    const float vf = tt ? (tb ? df[1][1] : df[1][0]) : (tb ? df[0][1] : df[0][0]);
    const float val = (tb ? gt[1] : gt[0]) * va + (tb ? gt[3] : gt[2]) * vf;
    const int n = (2 * w + tt) * 16 + col;
    const u32 off = WS_P2 + ((((cg * 2 + tb) * 36 + (n >> 3)) * 128) + u) * 64 + (n & 7) * 8;
    __builtin_amdgcn_raw_buffer_store_b64(u32x2{__float_as_uint(val), tag}, rws, off, 0, AUX_SC1);
    STAMP(12);
  }
  if (wg >= 144 || w >= 8) { STAMP_DUMP(); return; }
  {
    const u32 off = WS_P2 + (((fcg * 2 + fb_) * 36 + ch) * 128) * 64 + w * 1024 + lane * 16;
    u32x4 pg;
    u32 pf[4];
    wait_hint(p.ws, WS_F2 + (wg * 32 + (lane & 15)) * 4, tag, pf);
    STAMP(14);
    if (HDELAY2) delay_ticks(__builtin_amdgcn_s_memrealtime(), HDELAY2);
#pragma unroll 1
    for (u32 spin = 0;; ++spin) {
      asm volatile("" ::: "memory");
      pg = __builtin_amdgcn_raw_buffer_load_b128(rws, off, 0, AUX_SC1);
      if (__all((pg[1] == tag) & (pg[3] == tag)) || spin > SPIN_CAP) break;
      __builtin_amdgcn_s_sleep(RSLEEP);
    }
    KEEP_POLLS(pf);
    float t0 = __uint_as_float(pg[0]), t1 = __uint_as_float(pg[2]);
    t0 += dpp<ROR4>(t0); t0 += dpp<ROR8>(t0); t0 = x32_sum(x16_sum(t0));
    t1 += dpp<ROR4>(t1); t1 += dpp<ROR8>(t1); t1 = x32_sum(x16_sum(t1));
    if (lane < 4) *(LAS f32x2*)(L + L_FIN + w * 32 + lane * 8) = f32x2{t0, t1};
    __syncthreads();
    if (w == 0) {
      if (lane < 8) {
        float r = 0.f;
#pragma unroll
        for (int j = 0; j < 8; ++j) r += *(LAS float*)(L + L_FIN + j * 32 + lane * 4);
        r += ((const LAS float*)(L + L_G))[2 + fb_] * bias;
        p.out[fb_ * D + fcg * 288 + ch * 8 + lane] = (u16)(cvtpk(r, r) & 0xffffu);
      }
      if (wg == 0 && lane == 0) { __hip_atomic_store((GAS u32*)(p.ws + WS_E), tag, __ATOMIC_RELAXED, __HIP_MEMORY_SCOPE_AGENT);
        *(GAS u64*)(p.ws + WS_STAMP) = __builtin_amdgcn_s_memrealtime(); }
      STAMP(13);
    }
    STAMP_DUMP();
  }
}

__global__ void post_kernel(unsigned char* ws, u16* out) {
  const u64 t = __builtin_amdgcn_s_memrealtime();
  if (threadIdx.x == 0) {
    const u64 te = *(GAS u64*)(ws + WS_STAMP);
    const u32 gap = (u32)(t - te);
    if (gap > 1000u) { const float r = bf2f(out[0]) + (float)gap * 0.001f; out[0] = (u16)(cvtpk(r, r) & 0xffffu); }
  }
}
extern "C" void kernel_launch(void* const* d_in, const int* in_sizes, int n_in, void* d_out, int out_size, void* d_ws, size_t ws_size,
                              hipStream_t stream) {
  Params p;
  p.x = (const u16*)d_in[0]; p.K = (const u16*)d_in[1]; p.V = (const u16*)d_in[2]; p.wq = (const u16*)d_in[3]; p.wo = (const u16*)d_in[4];
  p.w1 = (const u16*)d_in[6]; p.b1 = (const u16*)d_in[7]; p.w2 = (const u16*)d_in[8]; p.b2 = (const u16*)d_in[9]; p.gw = (const u16*)d_in[10];
  p.out = (u16*)d_out; p.ws = (unsigned char*)d_ws;
  (void)hipFuncSetAttribute((const void*)fused_kernel, hipFuncAttributeMaxDynamicSharedMemorySize, L_END);
  hipLaunchKernelGGL(fused_kernel, dim3(NWG), dim3(NTHR), L_END, stream, p);
  hipLaunchKernelGGL(post_kernel, dim3(1), dim3(64), 0, stream, (unsigned char*)d_ws, (u16*)d_out);
}
```

```cpp
#include <hip/hip_runtime.h>
#include <stdint.h>

typedef unsigned short u16;
typedef unsigned int u32;
typedef unsigned long long u64;
typedef __attribute__((ext_vector_type(4))) unsigned u32x4;
typedef __attribute__((ext_vector_type(2))) unsigned u32x2;
typedef __attribute__((ext_vector_type(4))) float f32x4;
typedef __attribute__((ext_vector_type(2))) float f32x2;
typedef __attribute__((ext_vector_type(4))) short s16x4;
typedef __attribute__((ext_vector_type(2))) __bf16 bf16x2;
#define GAS __attribute__((address_space(1)))
#define LAS __attribute__((address_space(3)))

constexpr int D = 576, HKV = 16, SK = 32768, DH = 128, WIN = 4096, F = 4608;
constexpr int NWG = 256, NTHR = 1024;

constexpr u32 WS_E = 0;
constexpr u32 WS_QP = 256;
constexpr u32 WS_HP = WS_QP + 16 * 2 * 4 * 256 * 8;
constexpr u32 WS_P1 = WS_HP + 3 * 2 * F * 8;
constexpr u32 WS_P2 = WS_P1 + 16 * 8 * 16 * 288;
constexpr u32 WS_F1 = WS_P2 + 2 * 2 * 36 * 128 * 64;
constexpr u32 WS_F2 = WS_F1 + 16 * 16 * 128;
constexpr u32 WS_END = WS_F2 + 144 * 128;
constexpr u32 WS_STAMP = 64u << 20;

constexpr u32 L_XA = 0, L_WQ = 3072, L_W1 = L_WQ + 18432, L_WO = L_W1 + 24576, L_W2 = L_WO + 18432;
constexpr u32 L_MRG = L_W2 + 21504;
constexpr u32 L_MM = L_MRG + 16384;
constexpr u32 L_ML = L_MM + 128;
constexpr u32 L_QS = L_ML + 128;
constexpr u32 L_AO = L_QS + 512;
constexpr u32 L_AH = L_AO + 128;
constexpr u32 L_G = L_AH + 192;
constexpr u32 L_FLAG = L_G + 16;
constexpr u32 L_JUNK = L_FLAG + 128;
constexpr u32 L_END = L_JUNK + 256;
constexpr u32 FL_WQ = L_FLAG + 16, FL_W1 = L_FLAG + 32, FL_Q = L_FLAG + 48, FL_WD = L_FLAG + 64;

struct Params {
  const u16* x; const u16* K; const u16* V; const u16* wq; const u16* wo; const u16* w1;
  const u16* b1; const u16* w2; const u16* b2; const u16* gw; u16* out; unsigned char* ws;
};

constexpr int AUX_NT = 2, AUX_SC1 = 16;
#define W_NT 1
#define MAP 0
#define PIPE 0
#define XSHIFT 2
#define DRY 0
#define H2POS 0
#define HD2 0
#define RSLEEP2 4
#define PRETOUCH 1
#define QDELAY 220
#define RSLEEP 4
constexpr u32 SPIN_CAP = 1u << 16;
constexpr float LOG2E = 1.4426950408889634f;

__device__ __forceinline__ float bf2f(u32 v16) { return __uint_as_float(v16 << 16); }
__device__ __forceinline__ u32 cvtpk(float lo, float hi) {
  u32 r; asm("v_cvt_pk_bf16_f32 %0, %1, %2" : "=v"(r) : "v"(lo), "v"(hi)); return r;
}
__device__ __forceinline__ float rbf(float f) { return __uint_as_float(cvtpk(f, f) << 16); }
__device__ __forceinline__ float dot2(u32 a, u32 b, float acc) {
  return __builtin_amdgcn_fdot2_f32_bf16(__builtin_bit_cast(bf16x2, a), __builtin_bit_cast(bf16x2, b), acc, false);
}
template <int CTRL> __device__ __forceinline__ float dpp(float x) {
  return __builtin_bit_cast(float, __builtin_amdgcn_mov_dpp(__builtin_bit_cast(int, x), CTRL, 0xf, 0xf, true));
}
constexpr int XOR1 = 0xB1, XOR2 = 0x4E, XOR7 = 0x141, XOR8 = 0x128, ROR4 = 0x124, ROR8 = 0x128, BCAST = 0x150;
__device__ __forceinline__ float x16_max(float x) {
  auto s = __builtin_amdgcn_permlane16_swap(__float_as_uint(x), __float_as_uint(x), false, false);
  return fmaxf(__uint_as_float(s[0]), __uint_as_float(s[1]));
}
__device__ __forceinline__ float x32_max(float x) {
  auto s = __builtin_amdgcn_permlane32_swap(__float_as_uint(x), __float_as_uint(x), false, false);
  return fmaxf(__uint_as_float(s[0]), __uint_as_float(s[1]));
}
__device__ __forceinline__ float x16_sum(float x) {
  auto s = __builtin_amdgcn_permlane16_swap(__float_as_uint(x), __float_as_uint(x), false, false);
  return __uint_as_float(s[0]) + __uint_as_float(s[1]);
}
__device__ __forceinline__ float x32_sum(float x) {
  auto s = __builtin_amdgcn_permlane32_swap(__float_as_uint(x), __float_as_uint(x), false, false);
  return __uint_as_float(s[0]) + __uint_as_float(s[1]);
}
template <int NT> __device__ __forceinline__ void glds16(const void* sbase, u32 voff, u32 lds_dst) {
  unsigned keep;
  if (NT)
    asm volatile("s_mov_b32 %0, m0\n\ts_mov_b32 m0, %3\n\ts_nop 0\n\tglobal_load_lds_dwordx4 %1, %2 nt\n\ts_mov_b32 m0, %0" : "=&s"(keep) : "v"(voff), "s"(sbase), "s"(lds_dst) : "memory");
  else
    asm volatile("s_mov_b32 %0, m0\n\ts_mov_b32 m0, %3\n\ts_nop 0\n\tglobal_load_lds_dwordx4 %1, %2\n\ts_mov_b32 m0, %0" : "=&s"(keep) : "v"(voff), "s"(sbase), "s"(lds_dst) : "memory");
}
__device__ __forceinline__ f32x4 mfma16(s16x4 a, s16x4 b, f32x4 c) { return __builtin_amdgcn_mfma_f32_16x16x16bf16_1k(a, b, c, 0, 0, 0); }
__device__ __forceinline__ s16x4 lds_tr(LAS unsigned char* L, u32 off) { return __builtin_amdgcn_ds_read_tr16_b64_v4i16((LAS s16x4*)(L + off)); }
__device__ __forceinline__ s16x4 lds_b64(LAS unsigned char* L, u32 off) { return *(LAS s16x4*)(L + off); }
__device__ __forceinline__ u32 lds_flag(LAS unsigned char* L, u32 off) { return __hip_atomic_load((LAS u32*)(L + off), __ATOMIC_RELAXED, __HIP_MEMORY_SCOPE_WORKGROUP); }
__device__ __forceinline__ void lds_set(LAS unsigned char* L, u32 off, u32 v) { __hip_atomic_store((LAS u32*)(L + off), v, __ATOMIC_RELAXED, __HIP_MEMORY_SCOPE_WORKGROUP); }
__device__ __forceinline__ void lds_wait4(LAS unsigned char* L, u32 off) {
#pragma unroll 1
  for (u32 spin = 0; spin < (1u << 22); ++spin) {
    const u32 f = lds_flag(L, off + (__lane_id() & 3) * 4);
    if (__all(f != 0u)) break;
    __builtin_amdgcn_s_sleep(1);
  }
  asm volatile("" ::: "memory");
}
#define KEEP_POLLS(f) asm volatile("" :: "v"((f)[0]), "v"((f)[1]), "v"((f)[2]), "v"((f)[3]))
__device__ __forceinline__ void wait_hint(const unsigned char* ws, u32 off, u32 tag, u32 (&f)[4]) {
  u32 cnt;
  asm volatile(
      "s_mov_b32 %4, 0x10000\n"
      "1:\n\t"
      "global_load_dword %0, %5, %6 sc1\n\ts_waitcnt vmcnt(0)\n\tv_cmp_ne_u32 vcc, %7, %0\n\ts_cbranch_vccz 2f\n\t"
      "s_sub_u32 %4, %4, 1\n\ts_cmp_lg_u32 %4, 0\n\ts_cbranch_scc1 1b\n"
      "2:\n\tv_mov_b32 %1, 0\n\tv_mov_b32 %2, 0\n\tv_mov_b32 %3, 0"
      : "=&v"(f[0]), "=&v"(f[1]), "=&v"(f[2]), "=&v"(f[3]), "=&s"(cnt)
      : "v"(off), "s"(ws), "s"(tag)
      : "vcc", "scc", "memory");
}
__device__ __forceinline__ void delay_ticks(u64 t0, u32 n) { while (__builtin_amdgcn_s_memrealtime() - t0 < n) __builtin_amdgcn_s_sleep(1); }

#define STAMP_DUMP() do {} while (0)
#define STAMP(i) do {} while (0)

template <int WHICH> __device__ __forceinline__ void load_wo_w2(const Params& p, u32 lb, int lane, int j0, int nw, int hkv, int rb, int cg, int u) {
  if (WHICH & 1) {
  const char* bo = (const char*)p.wo + (size_t)(hkv * 256 + 32 * rb) * 1152 + cg * 576;
#pragma unroll 1
  for (int j = j0; j < 18; j += nw) { const u32 c = 64 * j + lane; const u32 row = (c * 1821u) >> 16; glds16<W_NT>(bo, row * 576 + c * 16, lb + L_WO + j * 1024); }
  }
  if (WHICH & 2) {
  const char* b2 = (const char*)p.w2 + (size_t)(36 * u) * 1152 + cg * 576;
#pragma unroll 1
  for (int j = j0; j < 21; j += nw) { const u32 c = 64 * j + lane; const u32 row = (c * 1821u) >> 16; if (c < 1296) glds16<W_NT>(b2, row * 576 + c * 16, lb + L_W2 + j * 1024); }
  }
}

__global__ void __launch_bounds__(NTHR, 4) fused_kernel(Params p) {
  extern __shared__ __attribute__((aligned(16))) unsigned char smem[];
  LAS unsigned char* L = (LAS unsigned char*)smem;
  const u32 lb = (u32)(uintptr_t)L;
  const int tid = threadIdx.x, lane = tid & 63, w = __builtin_amdgcn_readfirstlane(tid >> 6);
  const int wg = blockIdx.x;
  const int xcd = wg & 7, idx = wg >> 3, hkv = xcd * 2 + (idx >> 4), ii = idx & 15, b = ii >> 3, s = ii & 7;
  const int hc = (hkv + XSHIFT) & 15;
  const int rb = ii >> 1, cg = ii & 1, u = hc * 8 + rb;
  const __amdgpu_buffer_rsrc_t rws = __builtin_amdgcn_make_buffer_rsrc((void*)p.ws, 0, WS_END, 0x00020000);
  STAMP(0);
  const int g4 = lane >> 4, col = lane & 15, q4 = col >> 2, p4 = lane & 3;
  if (w < 4) {
    const int cb = ii & 3, kq = ii >> 2;
    const u32 voff = (lane >> 3) * 8192 + (lane & 7) * 16;
    const char* base = (const char*)p.wq + (size_t)(144 * kq + 8 * w) * 8192 + (hkv * 256 + 64 * cb) * 2;
    if (w < 2) glds16<0>(p.x, w * 1024 + lane * 16, lb + L_XA + w * 1024);
    else if (w == 2) { if (lane < 16) glds16<0>(p.x, 2048 + lane * 16, lb + L_XA + 2048); }
#pragma unroll 1
    for (int j = w; j < 18; j += 4) { glds16<W_NT>(base, voff, lb + L_WQ + j * 1024); base += 32 * 8192; }
  } else if (w == 15) {
    if (lane < 32) lds_set(L, L_FLAG + lane * 4, 0u);
    asm volatile("s_waitcnt lgkmcnt(0)" ::: "memory");
  }
  __builtin_amdgcn_s_barrier();
  u32 Eraw;
  asm volatile("s_load_dword %0, %1, 0x0" : "=s"(Eraw) : "s"(p.ws) : "memory");
  if (w >= 4 && w < 8) {
    const int w4 = w - 4;
    if (wg < 216) {
      const int cb = wg / 3, kt = wg - 3 * cb;
      const u32 voff = (lane >> 3) * 9216 + (lane & 7) * 16;
      const char* base = (const char*)p.w1 + (size_t)(192 * kt + 8 * w4) * 9216 + cb * 128;
#pragma unroll 1
      for (int j = w4; j < 24; j += 4) { glds16<W_NT>(base, voff, lb + L_W1 + j * 1024); base += 32 * 9216; }
    }
  }
  if (w == 14) {
    const int fbp = wg / 72, c72p = wg - 72 * fbp, fcgp = c72p / 36, chp = c72p - 36 * fcgp;
    u32 o;
    if (lane < 36) o = WS_P2 + (((cg * 2 + (lane >= 18)) * 36 + 2 * (lane >= 18 ? lane - 18 : lane)) * 128 + u) * 64;
    else if (lane < 44) o = WS_P2 + (((fcgp * 2 + (fbp & 1)) * 36 + chp) * 128) * 64 + (lane - 36) * 1024;
    else if (lane == 44) o = WS_F1 + (hc * 16 + ii) * 128;
    else if (lane == 45) o = WS_F2 + (wg < 144 ? wg : 0) * 128;
    else if (lane == 46) o = WS_P1 + ((hc * 8 + rb) * 16) * 288;
    else if (lane == 47) o = WS_P1 + ((hkv * 8) * 16 + b * 8 + s) * 288;
    else if (lane == 48) o = WS_F1 + hkv * 16 * 128;
    else o = WS_HP + 36 * u * 8;
    const unsigned char* a = lane == 63 ? (const unsigned char*)p.out : p.ws + o;
    unsigned keep;
    asm volatile("s_mov_b32 %0, m0\n\ts_mov_b32 m0, %2\n\ts_nop 0\n\tglobal_load_lds_dword %1, off sc1\n\ts_mov_b32 m0, %0" : "=&s"(keep) : "v"(a), "s"(lb + L_JUNK) : "memory");
  }
  u32x4 ggw[2];
  if (w == 8) {
    const GAS u32x4* gp = (const GAS u32x4*)p.gw;
    ggw[0] = gp[lane]; ggw[1] = gp[64 + (lane & 7)];
  }
  const __amdgpu_buffer_rsrc_t rk = __builtin_amdgcn_make_buffer_rsrc((void*)p.K, 0, 0x10000000, 0x00020000);
  const __amdgpu_buffer_rsrc_t rv = __builtin_amdgcn_make_buffer_rsrc((void*)p.V, 0, 0x10000000, 0x00020000);
  const u32 kvoff = (u32)(((u32)(b * HKV + hkv) * SK + (SK - WIN) + s * 512 + w * 32) * (DH * 2));
  u64 tpub = 0;
  if (w < 4) {
    asm volatile("s_waitcnt vmcnt(0) lgkmcnt(0)" : "+s"(Eraw) :: "memory");
    const u32 tag = Eraw + 1u;
    if (lane == 0) lds_set(L, FL_WQ + w * 4, 1u);
    lds_wait4(L, FL_WQ);
    STAMP(2);
    const int cb = ii & 3, kq = ii >> 2;
    const u32 aoff = L_XA + (lane & 1) * 1152 + (144 * kq + 4 * g4) * 2;
    const u32 boff = L_WQ + (4 * g4 + q4) * 128 + p4 * 8 + w * 32;
    s16x4 fa[9], fb[9];
#pragma unroll
    for (int st = 0; st < 9; ++st) { fa[st] = lds_b64(L, aoff + st * 32); fb[st] = lds_tr(L, boff + st * 2048); }
    f32x4 acc = {0.f, 0.f, 0.f, 0.f};
#pragma unroll
    for (int st = 0; st < 9; ++st) acc = mfma16(fa[st], fb[st], acc);
    if (lane < 32) {
      const u32 o0 = WS_QP + (((hkv * 2 + g4) * 4 + kq) * 256 + 64 * cb + 16 * w + col) * 8;
      __builtin_amdgcn_raw_buffer_store_b64(u32x2{__float_as_uint(g4 ? acc[1] : acc[0]), tag}, rws, o0, 0, AUX_SC1);
    }
    tpub = __builtin_amdgcn_s_memrealtime();
    STAMP(3);
  }
  u32x4 kk[8], vv[8];
#pragma unroll
  for (int j = 0; j < 8; ++j) kk[j] = __builtin_amdgcn_raw_buffer_load_b128(rk, lane * 16, kvoff + j * 1024, AUX_NT);
#pragma unroll
  for (int j = 0; j < 8; ++j) vv[j] = __builtin_amdgcn_raw_buffer_load_b128(rv, lane * 16, kvoff + j * 1024, AUX_NT);
  __builtin_amdgcn_sched_barrier(0);
  if (lane == 0) __hip_atomic_fetch_add((LAS u32*)(L + L_FLAG + 4), 1u, __ATOMIC_RELAXED, __HIP_MEMORY_SCOPE_WORKGROUP);
  STAMP(1);
  asm volatile("s_waitcnt lgkmcnt(0)" : "+s"(Eraw) :: "memory");
  const u32 tag = Eraw + 1u;
  if (w < 4) {
    const u32 go = WS_QP + (((hkv * 2 + b) * 4 + (lane >> 5)) * 256 + 64 * w + 2 * (lane & 31)) * 8;
    delay_ticks(tpub, QDELAY);
    u32x4 qa, qb;
#pragma unroll 1
    for (u32 spin = 0;; ++spin) {
      asm volatile("" ::: "memory");
      qa = __builtin_amdgcn_raw_buffer_load_b128(rws, go, 0, AUX_SC1);
      qb = __builtin_amdgcn_raw_buffer_load_b128(rws, go + 2 * 2048, 0, AUX_SC1);
      if (__all((qa[1] == tag) & (qa[3] == tag) & (qb[1] == tag) & (qb[3] == tag)) || spin > SPIN_CAP) break;
      __builtin_amdgcn_s_sleep(8);
    }
    const float c0 = x32_sum(__uint_as_float(qa[0]) + __uint_as_float(qb[0])), c1 = x32_sum(__uint_as_float(qa[2]) + __uint_as_float(qb[2]));
    if (lane < 32) *(LAS u32*)(L + L_QS + w * 128 + lane * 4) = cvtpk(c0, c1);
    asm volatile("s_waitcnt lgkmcnt(0)" ::: "memory");
    if (lane == 0) lds_set(L, FL_Q + w * 4, 1u);
    STAMP(4);
  } else if (w < 8) {
    asm volatile("s_waitcnt vmcnt(16)" ::: "memory");
    if (lane == 0) lds_set(L, FL_W1 + (w - 4) * 4, 1u);
    if (wg < 216) {
      lds_wait4(L, FL_WQ);
      lds_wait4(L, FL_W1);
      const int cb = wg / 3, kt = wg - 3 * cb;
      const u32 aoff = L_XA + (lane & 1) * 1152 + (192 * kt + 4 * g4) * 2;
      const u32 boff = L_W1 + (4 * g4 + q4) * 128 + p4 * 8 + (w - 4) * 32;
      f32x4 acc = {0.f, 0.f, 0.f, 0.f};
#pragma unroll
      for (int hf = 0; hf < 2; ++hf) {
        s16x4 fa[6], fb[6];
#pragma unroll
        for (int st = 0; st < 6; ++st) { fa[st] = lds_b64(L, aoff + (hf * 6 + st) * 32); fb[st] = lds_tr(L, boff + (hf * 6 + st) * 2048); }
#pragma unroll
        for (int st = 0; st < 6; ++st) acc = mfma16(fa[st], fb[st], acc);
      }
      if (lane < 32) {
        const u32 o0 = WS_HP + ((kt * 2 + g4) * F + 64 * cb + 16 * (w - 4) + col) * 8;
        __builtin_amdgcn_raw_buffer_store_b64(u32x2{__float_as_uint(g4 ? acc[1] : acc[0]), tag}, rws, o0, 0, AUX_SC1);
      }
    }
  } else if (w == 8) {
    lds_wait4(L, FL_WQ);
    float s0 = 0.f, s1 = 0.f;
#pragma unroll
    for (int r = 0; r < 2; ++r) {
      const u32 xo = L_XA + (r ? 64 + (lane & 7) : lane) * 16;
      const u32x4 x0 = *(LAS u32x4*)(L + xo), x1 = *(LAS u32x4*)(L + xo + 1152);
      float t0 = 0.f, t1 = 0.f;
#pragma unroll
      for (int j = 0; j < 4; ++j) {
        const float glo = bf2f(ggw[r][j] & 0xffffu), ghi = __uint_as_float(ggw[r][j] & 0xffff0000u);
        t0 += rbf(bf2f(x0[j] & 0xffffu) * glo) + rbf(__uint_as_float(x0[j] & 0xffff0000u) * ghi);
        t1 += rbf(bf2f(x1[j] & 0xffffu) * glo) + rbf(__uint_as_float(x1[j] & 0xffff0000u) * ghi);
      }
      if (r == 0 || lane < 8) { s0 += t0; s1 += t1; }
    }
    s0 += dpp<XOR1>(s0); s0 += dpp<XOR2>(s0); s0 += dpp<ROR4>(s0); s0 += dpp<ROR8>(s0); s0 = x32_sum(x16_sum(s0));
    s1 += dpp<XOR1>(s1); s1 += dpp<XOR2>(s1); s1 += dpp<ROR4>(s1); s1 += dpp<ROR8>(s1); s1 = x32_sum(x16_sum(s1));
    const float g0 = rbf(1.f / (1.f + __expf(-rbf(s0)))), g1 = rbf(1.f / (1.f + __expf(-rbf(s1))));
    if (lane == 0) *(LAS f32x4*)(L + L_G) = f32x4{g0, g1, rbf(1.f - g0), rbf(1.f - g1)};
  }

  lds_wait4(L, FL_Q);
  const u32x4 q0 = *(LAS u32x4*)(L + L_QS + col * 16), q1 = *(LAS u32x4*)(L + L_QS + 256 + col * 16);
  STAMP(5);
  float x[16];
#pragma unroll
  for (int i = 0; i < 8; ++i) {
    float d0 = 0.f, d1 = 0.f;
#pragma unroll
    for (int j = 0; j < 4; ++j) { d0 = dot2(kk[i][j], q0[j], d0); d1 = dot2(kk[i][j], q1[j], d1); }
    x[2 * i] = d0; x[2 * i + 1] = d1;
  }
#define TR_STEP(HALF, CTRL, BIT) _Pragma("unroll") for (int h = 0; h < (HALF); ++h) { \
    const float lo_ = x[h], hi_ = x[h + (HALF)]; const float t1_ = lo_ + dpp<CTRL>(lo_), t2_ = hi_ + dpp<CTRL>(hi_); \
    x[h] = (col & (BIT)) ? t2_ : t1_; }
  TR_STEP(8, XOR8, 8) TR_STEP(4, XOR7, 4) TR_STEP(2, XOR2, 2) TR_STEP(1, XOR1, 1)
#undef TR_STEP
  const float sc = rbf(rbf(x[0]) * 0.08838834764831845f) * LOG2E;
  float mx = sc;
  mx = fmaxf(mx, dpp<XOR2>(mx)); mx = fmaxf(mx, dpp<ROR4>(mx)); mx = fmaxf(mx, dpp<ROR8>(mx)); mx = x32_max(x16_max(mx));
  const float pr = __builtin_amdgcn_exp2f(sc - mx);
  STAMP(6);
  f32x2 a0[4], a1[4];
#pragma unroll
  for (int j = 0; j < 4; ++j) { a0[j] = f32x2{0.f, 0.f}; a1[j] = f32x2{0.f, 0.f}; }
#define PV(i) { const float p0_ = dpp<BCAST + 2 * (i)>(pr), p1_ = dpp<BCAST + 2 * (i) + 1>(pr); \
    _Pragma("unroll") for (int j = 0; j < 4; ++j) { const f32x2 vf = {__uint_as_float(vv[i][j] << 16), __uint_as_float(vv[i][j] & 0xffff0000u)}; \
      a0[j] = __builtin_elementwise_fma(vf, f32x2{p0_, p0_}, a0[j]); a1[j] = __builtin_elementwise_fma(vf, f32x2{p1_, p1_}, a1[j]); } }
  float ls = pr;
  ls += dpp<XOR2>(ls); ls += dpp<ROR4>(ls); ls += dpp<ROR8>(ls); ls = x32_sum(x16_sum(ls));
  if (lane < 2) { *(LAS float*)(L + L_MM + lane * 64 + w * 4) = mx; *(LAS float*)(L + L_ML + lane * 64 + w * 4) = ls; }
  PV(0) PV(1) PV(2) PV(3) PV(4) PV(5) PV(6) PV(7)
#undef PV
  float y[8];
#pragma unroll
  for (int k = 0; k < 8; ++k) {
    auto r = __builtin_amdgcn_permlane32_swap(__float_as_uint(a0[k >> 1][k & 1]), __float_as_uint(a1[k >> 1][k & 1]), false, false);
    y[k] = __uint_as_float(r[0]) + __uint_as_float(r[1]);
  }
  f32x4 z;
#pragma unroll
  for (int k = 0; k < 4; ++k) {
    auto r = __builtin_amdgcn_permlane16_swap(__float_as_uint(y[k]), __float_as_uint(y[k + 4]), false, false);
    z[k] = __uint_as_float(r[0]) + __uint_as_float(r[1]);
  }
  *(LAS f32x4*)(L + L_MRG + w * 1024 + ((g4 >> 1) * 128 + 8 * col + 4 * (g4 & 1)) * 4) = z;
  asm volatile("s_waitcnt lgkmcnt(0)" ::: "memory");
  if (lane == 0) __hip_atomic_fetch_add((LAS u32*)(L + L_FLAG + 8), 1u, __ATOMIC_RELAXED, __HIP_MEMORY_SCOPE_WORKGROUP);
  STAMP(7);
  if (w == 5) {
    const int cc = lane < 36 ? lane : 35;
    const u32 ho = WS_HP + (36 * u + cc) * 8;
    u32x2 hg[6];
    const float bb = bf2f(p.b1[36 * u + cc]);
#pragma unroll 1
    for (u32 spin = 0;; ++spin) {
      asm volatile("" ::: "memory");
#pragma unroll
      for (int j = 0; j < 6; ++j) hg[j] = __builtin_amdgcn_raw_buffer_load_b64(rws, ho + j * F * 8, 0, AUX_SC1);
      bool ok = true;
#pragma unroll
      for (int j = 0; j < 6; ++j) ok &= hg[j][1] == tag;
      if (__all(ok) || spin > SPIN_CAP) break;
      __builtin_amdgcn_s_sleep(16);
    }
#pragma unroll
    for (int bb2 = 0; bb2 < 2; ++bb2) {
      float hs = __uint_as_float(hg[bb2][0]) + __uint_as_float(hg[2 + bb2][0]) + __uint_as_float(hg[4 + bb2][0]);
      hs = rbf(rbf(hs) + bb);
      hs = hs > 0.f ? hs : 0.f;
      const u32 hv = lane < 36 ? (cvtpk(hs * hs, 0.f) & 0xffffu) : 0u;
      if (lane < 48) *(LAS u16*)(L + L_AH + bb2 * 96 + lane * 2) = (u16)hv;
    }
  }
  const int fb_ = wg / 72, c72 = wg - 72 * fb_, fcg = c72 / 36, ch = c72 - 36 * fcg;
  u32 bias2 = 0;
  if (w == 0 && wg < 144 && lane < 4) bias2 = *(const GAS u32*)(p.b2 + fcg * 288 + ch * 8 + 2 * lane);
  if (w >= 5 && w < 9) {
#pragma unroll 1
    for (u32 spin = 0; spin < (1u << 22); ++spin) {
      if (lds_flag(L, L_FLAG + 4) == 16u) break;
      __builtin_amdgcn_s_sleep(1);
    }
    load_wo_w2<3>(p, lb, lane, w - 5, 4, hc, rb, cg, u);
    STAMP(3);
    asm volatile("s_waitcnt vmcnt(0) lgkmcnt(0)" ::: "memory");
    if (lane == 0) lds_set(L, FL_WD + (w - 5) * 4, 1u);
    STAMP(10);
  }
  if (w < 4) {
    const int h = w >> 1, d = tid & 127;
    const u32 rec = WS_P1 + (((hkv * 8 + h * 4 + (d >> 5)) * 16) + b * 8 + s) * 288;
#pragma unroll 1
    for (int pass = DRY ? 0 : 1; pass < 2; ++pass) {
      asm volatile("" : "+s"(pass));
      if (pass) {
#pragma unroll 1
        for (u32 spin = 0; spin < (1u << 22); ++spin) {
          if (lds_flag(L, L_FLAG + 8) == 16u) break;
        }
        asm volatile("" ::: "memory");
        if (lane < 4) __builtin_amdgcn_raw_buffer_store_b32(tag, rws, WS_F1 + ((hkv * 16 + w * 4 + lane) * 32 + ii) * 4, 0, AUX_SC1);
        STAMP(8);
      }
      float ov[16];
#pragma unroll
      for (int j = 0; j < 16; ++j) ov[j] = *(LAS float*)(L + L_MRG + j * 1024 + tid * 4);
      const float mj = *(LAS float*)(L + L_MM + h * 64 + col * 4), lj = *(LAS float*)(L + L_ML + h * 64 + col * 4);
      float M = mj;
      M = fmaxf(M, dpp<XOR1>(M)); M = fmaxf(M, dpp<XOR2>(M)); M = fmaxf(M, dpp<ROR4>(M)); M = fmaxf(M, dpp<ROR8>(M));
      const float e = __builtin_amdgcn_exp2f(mj - M);
      float Ls = e * lj;
      Ls += dpp<XOR1>(Ls); Ls += dpp<XOR2>(Ls); Ls += dpp<ROR4>(Ls); Ls += dpp<ROR8>(Ls);
      float o = 0.f;
#pragma unroll
      for (int j = 0; j < 16; ++j) o = fmaf(__uint_as_float(__builtin_amdgcn_readlane(__float_as_uint(e), j)), ov[j], o);
      asm volatile("" :: "v"(o), "v"(Ls));
      if (pass) {
        __builtin_amdgcn_raw_buffer_store_b64(u32x2{__float_as_uint(o), tag}, rws, rec + (d & 31) * 8, 0, AUX_SC1);
        if ((d & 31) < 2) __builtin_amdgcn_raw_buffer_store_b64(u32x2{__float_as_uint((d & 1) ? Ls : M), tag}, rws, rec + 256 + (d & 1) * 8, 0, AUX_SC1);
        STAMP(9);
      }
    }
  } else if (w == 4) {
    const int gb = lane >> 5, gs = (lane >> 2) & 7, dq = lane & 3;
    const u32 rec = WS_P1 + (((hc * 8 + rb) * 16) + gb * 8 + gs) * 288;
#pragma unroll 1
    for (int pass = DRY ? 0 : 1; pass < 2; ++pass) {
      asm volatile("" : "+s"(pass));
      u32x4 og[4], ml;
      if (pass) {
        u32 pf[4];
#pragma unroll 1
        for (u32 spin = 0; spin < (1u << 22); ++spin) { if (lds_flag(L, L_FLAG + 8) == 16u) break; __builtin_amdgcn_s_sleep(1); }
        wait_hint(p.ws, WS_F1 + ((hc * 16 + ii) * 32 + (lane & 15)) * 4, tag, pf);
#define SEND_HINT2() if (rb == 0) { \
          __builtin_amdgcn_raw_buffer_store_b32(tag, rws, WS_F2 + (((lane & 1) * 72 + cg * 36 + (lane >> 1)) * 32 + hc) * 4, 0, AUX_SC1); \
          if (lane < 8) __builtin_amdgcn_raw_buffer_store_b32(tag, rws, WS_F2 + (((lane & 1) * 72 + cg * 36 + 32 + (lane >> 1)) * 32 + hc) * 4, 0, AUX_SC1); }
        SEND_HINT2()
        STAMP(15);
#pragma unroll 1
        for (u32 spin = 0;; ++spin) {
          asm volatile("" ::: "memory");
#pragma unroll
          for (int j = 0; j < 4; ++j) og[j] = __builtin_amdgcn_raw_buffer_load_b128(rws, rec + dq * 64 + j * 16, 0, AUX_SC1);
          ml = __builtin_amdgcn_raw_buffer_load_b128(rws, rec + 256, 0, AUX_SC1);
          bool ok = (ml[1] == tag) & (ml[3] == tag);
#pragma unroll
          for (int j = 0; j < 4; ++j) ok &= (og[j][1] == tag) & (og[j][3] == tag);
          if (__all(ok) || spin > SPIN_CAP) break;
          __builtin_amdgcn_s_sleep(RSLEEP);
        }
        KEEP_POLLS(pf);
      } else {
#pragma unroll
        for (int j = 0; j < 4; ++j) og[j] = q0;
        ml = q1;
      }
      const float m = __uint_as_float(ml[0]);
      float M = m;
      M = fmaxf(M, dpp<ROR4>(M)); M = fmaxf(M, dpp<ROR8>(M)); M = x16_max(M);
      const float e = __builtin_amdgcn_exp2f(m - M);
      float Ls = e * __uint_as_float(ml[2]);
      Ls += dpp<ROR4>(Ls); Ls += dpp<ROR8>(Ls); Ls = x16_sum(Ls);
      const float inv = 1.f / Ls;
      float ov[8];
#pragma unroll
      for (int k = 0; k < 8; ++k) {
        float t = e * __uint_as_float(og[k >> 1][(k & 1) * 2]);
        t += dpp<ROR4>(t); t += dpp<ROR8>(t); t = x16_sum(t);
        ov[k] = t * inv;
      }
      if (gs == 0) *(LAS u32x4*)(L + L_AO + gb * 64 + dq * 16) = u32x4{cvtpk(ov[0], ov[1]), cvtpk(ov[2], ov[3]), cvtpk(ov[4], ov[5]), cvtpk(ov[6], ov[7])};
      if (pass) STAMP(10);
    }
  }
  if (w < 10 && w != 4) {
    const int mw = w < 4 ? w : w - 1;
    const u32 ao = L_AO + (lane & 1) * 64 + 4 * g4 * 2, ah = L_AH + (lane & 1) * 96 + 4 * g4 * 2;
    const int rr = 4 * g4 + q4, tb = g4 & 1, tt = g4 >> 1;
    lds_wait4(L, FL_WD);
    f32x4 df[2];
    s16x4 bo[2][2];
#pragma unroll
    for (int t = 0; t < 2; ++t) df[t] = f32x4{0.f, 0.f, 0.f, 0.f};
#pragma unroll
    for (int st = 0; st < 3; ++st) {
      const s16x4 a = lds_b64(L, ah + st * 32);
      const int row = min(16 * st + rr, 35);
#pragma unroll
      for (int t = 0; t < 2; ++t) df[t] = mfma16(a, lds_tr(L, L_W2 + row * 576 + (2 * mw + t) * 32 + p4 * 8), df[t]);
    }
#pragma unroll
    for (int st = 0; st < 2; ++st)
#pragma unroll
      for (int t = 0; t < 2; ++t) bo[st][t] = lds_tr(L, L_WO + (16 * st + rr) * 576 + (2 * mw + t) * 32 + p4 * 8);
    const f32x4 gt = *(const LAS f32x4*)(L + L_G);
    const float ga = tb ? gt[1] : gt[0];
    const float vf = (tb ? gt[3] : gt[2]) * (tt ? (tb ? df[1][1] : df[1][0]) : (tb ? df[0][1] : df[0][0]));
    const int n = (2 * mw + tt) * 16 + col;
    const u32 off = WS_P2 + ((((cg * 2 + tb) * 36 + (n >> 3)) * 128) + u) * 64 + (n & 7) * 8;
#pragma unroll 1
    for (int pass = DRY ? 0 : 1; pass < 2; ++pass) {
      asm volatile("" : "+s"(pass));
      if (pass) { asm volatile("s_waitcnt lgkmcnt(0)\n\ts_barrier" ::: "memory"); STAMP(11); }
      f32x4 da[2];
#pragma unroll
      for (int t = 0; t < 2; ++t) da[t] = f32x4{0.f, 0.f, 0.f, 0.f};
#pragma unroll
      for (int st = 0; st < 2; ++st) {
        const s16x4 a = lds_b64(L, ao + st * 32);
#pragma unroll
        for (int t = 0; t < 2; ++t) da[t] = mfma16(a, bo[st][t], da[t]);
      }
      const float val = ga * (tt ? (tb ? da[1][1] : da[1][0]) : (tb ? da[0][1] : da[0][0])) + vf;
      asm volatile("" :: "v"(val));
      if (pass) { __builtin_amdgcn_raw_buffer_store_b64(u32x2{__float_as_uint(val), tag}, rws, off, 0, AUX_SC1); STAMP(12); }
    }
  } else {
    asm volatile("s_waitcnt lgkmcnt(0)\n\ts_barrier" ::: "memory");
  }
  if (wg >= 144 || w != 0) { STAMP_DUMP(); return; }
  {
    const u32 off = WS_P2 + (((fcg * 2 + fb_) * 36 + ch) * 128) * 64 + lane * 16;
    u32x4 pg[8];
    u32 pf[4];
    wait_hint(p.ws, WS_F2 + (wg * 32 + (lane & 15)) * 4, tag, pf);
    STAMP(14);
    if (HD2) delay_ticks(__builtin_amdgcn_s_memrealtime(), HD2);
#pragma unroll 1
    for (u32 spin = 0;; ++spin) {
      asm volatile("" ::: "memory");
#pragma unroll
      for (int j = 0; j < 8; ++j) pg[j] = __builtin_amdgcn_raw_buffer_load_b128(rws, off + j * 1024, 0, AUX_SC1);
      bool ok = true;
#pragma unroll
      for (int j = 0; j < 8; ++j) ok &= (pg[j][1] == tag) & (pg[j][3] == tag);
      if (__all(ok) || spin > SPIN_CAP) break;
      __builtin_amdgcn_s_sleep(RSLEEP2);
    }
    KEEP_POLLS(pf);
    float t0 = 0.f, t1 = 0.f;
#pragma unroll
    for (int j = 0; j < 8; ++j) { t0 += __uint_as_float(pg[j][0]); t1 += __uint_as_float(pg[j][2]); }
    t0 += dpp<ROR4>(t0); t0 += dpp<ROR8>(t0); t0 = x32_sum(x16_sum(t0));
    t1 += dpp<ROR4>(t1); t1 += dpp<ROR8>(t1); t1 = x32_sum(x16_sum(t1));
    const float og_ = ((const LAS float*)(L + L_G))[2 + fb_];
    t0 += og_ * bf2f(bias2 & 0xffffu); t1 += og_ * __uint_as_float(bias2 & 0xffff0000u);
    if (lane < 4) *(GAS u32*)(p.out + fb_ * D + fcg * 288 + ch * 8 + 2 * lane) = cvtpk(t0, t1);
    if (wg == 0 && lane == 0) __hip_atomic_store((GAS u32*)(p.ws + WS_E), tag, __ATOMIC_RELAXED, __HIP_MEMORY_SCOPE_AGENT);
    STAMP(13);
    STAMP_DUMP();
  }
}

extern "C" void kernel_launch(void* const* d_in, const int* in_sizes, int n_in, void* d_out, int out_size, void* d_ws, size_t ws_size,
                              hipStream_t stream) {
  Params p;
  p.x = (const u16*)d_in[0]; p.K = (const u16*)d_in[1]; p.V = (const u16*)d_in[2]; p.wq = (const u16*)d_in[3]; p.wo = (const u16*)d_in[4];
  p.w1 = (const u16*)d_in[6]; p.b1 = (const u16*)d_in[7]; p.w2 = (const u16*)d_in[8]; p.b2 = (const u16*)d_in[9]; p.gw = (const u16*)d_in[10];
  p.out = (u16*)d_out; p.ws = (unsigned char*)d_ws;
  (void)hipFuncSetAttribute((const void*)fused_kernel, hipFuncAttributeMaxDynamicSharedMemorySize, L_END);
  hipLaunchKernelGGL(fused_kernel, dim3(NWG), dim3(NTHR), L_END, stream, p);
}
```

```cpp
#include <hip/hip_runtime.h>
#include <stdint.h>

typedef unsigned short u16;
typedef unsigned int u32;
typedef unsigned long long u64;
typedef __attribute__((ext_vector_type(4))) unsigned u32x4;
typedef __attribute__((ext_vector_type(2))) unsigned u32x2;
typedef __attribute__((ext_vector_type(4))) float f32x4;
typedef __attribute__((ext_vector_type(2))) float f32x2;
typedef __attribute__((ext_vector_type(4))) short s16x4;
typedef __attribute__((ext_vector_type(2))) __bf16 bf16x2;
#define GAS __attribute__((address_space(1)))
#define LAS __attribute__((address_space(3)))

constexpr int D = 576, HKV = 16, SK = 32768, DH = 128, WIN = 4096, F = 4608;
constexpr int NWG = 256, NTHR = 1024;

constexpr u32 WS_E = 0;
constexpr u32 WS_QP = 256;
constexpr u32 WS_HP = WS_QP + 16 * 2 * 4 * 256 * 8;
constexpr u32 WS_P1 = WS_HP + 3 * 2 * F * 8;
constexpr u32 WS_P2 = WS_P1 + 16 * 8 * 16 * 288;
constexpr u32 WS_F1 = WS_P2 + 2 * 2 * 36 * 128 * 64;
constexpr u32 WS_F2 = WS_F1 + 16 * 16 * 128;
constexpr u32 WS_END = WS_F2 + 144 * 128;
constexpr u32 WS_STAMP = 64u << 20;

constexpr u32 L_XA = 0, L_WQ = 3072, L_W1 = L_WQ + 18432, L_WO = L_W1 + 24576, L_W2 = L_WO + 18432;
constexpr u32 L_MRG = L_W2 + 21504;
constexpr u32 L_MM = L_MRG + 16384;
constexpr u32 L_ML = L_MM + 128;
constexpr u32 L_QS = L_ML + 128;
constexpr u32 L_AO = L_QS + 512;
constexpr u32 L_AH = L_AO + 128;
constexpr u32 L_G = L_AH + 192;
constexpr u32 L_FLAG = L_G + 16;
constexpr u32 L_JUNK = L_FLAG + 128;
constexpr u32 L_END = L_JUNK + 256;
constexpr u32 FL_WQ = L_FLAG + 16, FL_W1 = L_FLAG + 32, FL_Q = L_FLAG + 48, FL_WD = L_FLAG + 64;

struct Params {
  const u16* x; const u16* K; const u16* V; const u16* wq; const u16* wo; const u16* w1;
  const u16* b1; const u16* w2; const u16* b2; const u16* gw; u16* out; unsigned char* ws;
};

constexpr int AUX_NT = 2, AUX_SC1 = 16;
#define W_NT 1
#define MAP 0
#define PIPE 0
#define XSHIFT 2
#define DRY 0
#define H2POS 0
#define HD2 0
#define RSLEEP2 4
#define PRETOUCH 1
#define QDELAY 220
#define RSLEEP 4
constexpr u32 SPIN_CAP = 1u << 16;
constexpr float LOG2E = 1.4426950408889634f;

__device__ __forceinline__ float bf2f(u32 v16) { return __uint_as_float(v16 << 16); }
__device__ __forceinline__ u32 cvtpk(float lo, float hi) {
  u32 r; asm("v_cvt_pk_bf16_f32 %0, %1, %2" : "=v"(r) : "v"(lo), "v"(hi)); return r;
}
__device__ __forceinline__ float rbf(float f) { return __uint_as_float(cvtpk(f, f) << 16); }
__device__ __forceinline__ float dot2(u32 a, u32 b, float acc) {
  return __builtin_amdgcn_fdot2_f32_bf16(__builtin_bit_cast(bf16x2, a), __builtin_bit_cast(bf16x2, b), acc, false);
}
template <int CTRL> __device__ __forceinline__ float dpp(float x) {
  return __builtin_bit_cast(float, __builtin_amdgcn_mov_dpp(__builtin_bit_cast(int, x), CTRL, 0xf, 0xf, true));
}
constexpr int XOR1 = 0xB1, XOR2 = 0x4E, XOR7 = 0x141, XOR8 = 0x128, ROR4 = 0x124, ROR8 = 0x128, BCAST = 0x150;
__device__ __forceinline__ float x16_max(float x) {
  auto s = __builtin_amdgcn_permlane16_swap(__float_as_uint(x), __float_as_uint(x), false, false);
  return fmaxf(__uint_as_float(s[0]), __uint_as_float(s[1]));
}
__device__ __forceinline__ float x32_max(float x) {
  auto s = __builtin_amdgcn_permlane32_swap(__float_as_uint(x), __float_as_uint(x), false, false);
  return fmaxf(__uint_as_float(s[0]), __uint_as_float(s[1]));
}
__device__ __forceinline__ float x16_sum(float x) {
  auto s = __builtin_amdgcn_permlane16_swap(__float_as_uint(x), __float_as_uint(x), false, false);
  return __uint_as_float(s[0]) + __uint_as_float(s[1]);
}
__device__ __forceinline__ float x32_sum(float x) {
  auto s = __builtin_amdgcn_permlane32_swap(__float_as_uint(x), __float_as_uint(x), false, false);
  return __uint_as_float(s[0]) + __uint_as_float(s[1]);
}
template <int NT> __device__ __forceinline__ void glds16(const void* sbase, u32 voff, u32 lds_dst) {
  unsigned keep;
  if (NT)
    asm volatile("s_mov_b32 %0, m0\n\ts_mov_b32 m0, %3\n\ts_nop 0\n\tglobal_load_lds_dwordx4 %1, %2 nt\n\ts_mov_b32 m0, %0" : "=&s"(keep) : "v"(voff), "s"(sbase), "s"(lds_dst) : "memory");
  else
    asm volatile("s_mov_b32 %0, m0\n\ts_mov_b32 m0, %3\n\ts_nop 0\n\tglobal_load_lds_dwordx4 %1, %2\n\ts_mov_b32 m0, %0" : "=&s"(keep) : "v"(voff), "s"(sbase), "s"(lds_dst) : "memory");
}
__device__ __forceinline__ f32x4 mfma16(s16x4 a, s16x4 b, f32x4 c) { return __builtin_amdgcn_mfma_f32_16x16x16bf16_1k(a, b, c, 0, 0, 0); }
__device__ __forceinline__ s16x4 lds_tr(LAS unsigned char* L, u32 off) { return __builtin_amdgcn_ds_read_tr16_b64_v4i16((LAS s16x4*)(L + off)); }
__device__ __forceinline__ s16x4 lds_b64(LAS unsigned char* L, u32 off) { return *(LAS s16x4*)(L + off); }
__device__ __forceinline__ u32 lds_flag(LAS unsigned char* L, u32 off) { return __hip_atomic_load((LAS u32*)(L + off), __ATOMIC_RELAXED, __HIP_MEMORY_SCOPE_WORKGROUP); }
__device__ __forceinline__ void lds_set(LAS unsigned char* L, u32 off, u32 v) { __hip_atomic_store((LAS u32*)(L + off), v, __ATOMIC_RELAXED, __HIP_MEMORY_SCOPE_WORKGROUP); }
__device__ __forceinline__ void lds_wait4(LAS unsigned char* L, u32 off) {
#pragma unroll 1
  for (u32 spin = 0; spin < (1u << 22); ++spin) {
    const u32 f = lds_flag(L, off + (__lane_id() & 3) * 4);
    if (__all(f != 0u)) break;
    __builtin_amdgcn_s_sleep(1);
  }
  asm volatile("" ::: "memory");
}
#define KEEP_POLLS(f) asm volatile("" :: "v"((f)[0]), "v"((f)[1]), "v"((f)[2]), "v"((f)[3]))
__device__ __forceinline__ void wait_hint(const unsigned char* ws, u32 off, u32 tag, u32 (&f)[4]) {
  u32 cnt;
  asm volatile(
      "s_mov_b32 %4, 0x10000\n"
      "1:\n\t"
      "global_load_dword %0, %5, %6 sc1\n\ts_waitcnt vmcnt(0)\n\tv_cmp_ne_u32 vcc, %7, %0\n\ts_cbranch_vccz 2f\n\t"
      "s_sub_u32 %4, %4, 1\n\ts_cmp_lg_u32 %4, 0\n\ts_cbranch_scc1 1b\n"
      "2:\n\tv_mov_b32 %1, 0\n\tv_mov_b32 %2, 0\n\tv_mov_b32 %3, 0"
      : "=&v"(f[0]), "=&v"(f[1]), "=&v"(f[2]), "=&v"(f[3]), "=&s"(cnt)
      : "v"(off), "s"(ws), "s"(tag)
      : "vcc", "scc", "memory");
}
__device__ __forceinline__ void delay_ticks(u64 t0, u32 n) { while (__builtin_amdgcn_s_memrealtime() - t0 < n) __builtin_amdgcn_s_sleep(1); }

#define STAMP_DUMP() do {} while (0)
#define STAMP(i) do {} while (0)

template <int WHICH> __device__ __forceinline__ void load_wo_w2(const Params& p, u32 lb, int lane, int j0, int nw, int hkv, int rb, int cg, int u) {
  if (WHICH & 1) {
  const char* bo = (const char*)p.wo + (size_t)(hkv * 256 + 32 * rb) * 1152 + cg * 576;
#pragma unroll 1
  for (int j = j0; j < 18; j += nw) { const u32 c = 64 * j + lane; const u32 row = (c * 1821u) >> 16; glds16<W_NT>(bo, row * 576 + c * 16, lb + L_WO + j * 1024); }
  }
  if (WHICH & 2) {
  const char* b2 = (const char*)p.w2 + (size_t)(36 * u) * 1152 + cg * 576;
#pragma unroll 1
  for (int j = j0; j < 21; j += nw) { const u32 c = 64 * j + lane; const u32 row = (c * 1821u) >> 16; if (c < 1296) glds16<W_NT>(b2, row * 576 + c * 16, lb + L_W2 + j * 1024); }
  }
}

__global__ void __launch_bounds__(NTHR, 4) fused_kernel(Params p) {
  extern __shared__ __attribute__((aligned(16))) unsigned char smem[];
  LAS unsigned char* L = (LAS unsigned char*)smem;
  const u32 lb = (u32)(uintptr_t)L;
  const int tid = threadIdx.x, lane = tid & 63, w = __builtin_amdgcn_readfirstlane(tid >> 6);
  const int wg = blockIdx.x;
  const int xcd = wg & 7, idx = wg >> 3, hkv = xcd * 2 + (idx >> 4), ii = idx & 15, b = ii >> 3, s = ii & 7;
  const int hc = (hkv + XSHIFT) & 15;
  const int rb = ii >> 1, cg = ii & 1, u = hc * 8 + rb;
  const __amdgpu_buffer_rsrc_t rws = __builtin_amdgcn_make_buffer_rsrc((void*)p.ws, 0, WS_END, 0x00020000);
  STAMP(0);
  const int g4 = lane >> 4, col = lane & 15, q4 = col >> 2, p4 = lane & 3;
  if (w < 4) {
    const int cb = ii & 3, kq = ii >> 2;
    const u32 voff = (lane >> 3) * 8192 + (lane & 7) * 16;
    const char* base = (const char*)p.wq + (size_t)(144 * kq + 8 * w) * 8192 + (hkv * 256 + 64 * cb) * 2;
    if (w < 2) glds16<0>(p.x, w * 1024 + lane * 16, lb + L_XA + w * 1024);
    else if (w == 2) { if (lane < 16) glds16<0>(p.x, 2048 + lane * 16, lb + L_XA + 2048); }
#pragma unroll 1
    for (int j = w; j < 18; j += 4) { glds16<W_NT>(base, voff, lb + L_WQ + j * 1024); base += 32 * 8192; }
  } else if (w == 15) {
    if (lane < 32) lds_set(L, L_FLAG + lane * 4, 0u);
    asm volatile("s_waitcnt lgkmcnt(0)" ::: "memory");
  }
  __builtin_amdgcn_s_barrier();
  u32 Eraw;
  asm volatile("s_load_dword %0, %1, 0x0" : "=s"(Eraw) : "s"(p.ws) : "memory");
  if (w >= 4 && w < 8) {
    const int w4 = w - 4;
    if (wg < 216) {
      const int cb = wg / 3, kt = wg - 3 * cb;
      const u32 voff = (lane >> 3) * 9216 + (lane & 7) * 16;
      const char* base = (const char*)p.w1 + (size_t)(192 * kt + 8 * w4) * 9216 + cb * 128;
#pragma unroll 1
      for (int j = w4; j < 24; j += 4) { glds16<W_NT>(base, voff, lb + L_W1 + j * 1024); base += 32 * 9216; }
    }
  }
  if (w == 14) {
    const int fbp = wg / 72, c72p = wg - 72 * fbp, fcgp = c72p / 36, chp = c72p - 36 * fcgp;
    u32 o = WS_HP + 36 * u * 8;
    if (lane < 36) o = WS_P2 + (((cg * 2 + (lane >= 18)) * 36 + 2 * (lane >= 18 ? lane - 18 : lane)) * 128 + u) * 64;
    else if (lane < 44) o = WS_P2 + (((fcgp * 2 + (fbp & 1)) * 36 + chp) * 128) * 64 + (lane - 36) * 1024;
    else if (lane == 44) o = WS_F1 + (hc * 16 + ii) * 128;
    else if (lane == 45) o = WS_F2 + (wg < 144 ? wg : 0) * 128;
    else if (lane == 46) o = WS_P1 + ((hc * 8 + rb) * 16) * 288;
    else if (lane == 47) o = WS_P1 + ((hkv * 8) * 16 + b * 8 + s) * 288;
    else if (lane == 48) o = WS_P1 + ((hkv * 8 + 7) * 16 + b * 8 + s) * 288;
    else if (lane == 49) o = WS_F1 + hkv * 16 * 128;
    const unsigned char* a = p.ws + o;
    if (lane >= 52 && lane < 56) a = (const unsigned char*)p.wo + (size_t)(hc * 256 + 32 * rb + (lane - 52) * 10) * 1152 + cg * 576;
    else if (lane >= 56 && lane < 60) a = (const unsigned char*)p.w2 + (size_t)(36 * u + (lane - 56) * 11) * 1152 + cg * 576;
    else if (lane == 60) a = (const unsigned char*)(p.b1 + 36 * u);
    else if (lane == 61) a = (const unsigned char*)(p.b2 + fcgp * 288 + chp * 8);
    else if (lane == 62) a = (const unsigned char*)(p.out + (fbp & 1) * D + fcgp * 288 + chp * 8);
    unsigned keep;
    asm volatile("s_mov_b32 %0, m0\n\ts_mov_b32 m0, %2\n\ts_nop 0\n\tglobal_load_lds_dword %1, off sc1\n\ts_mov_b32 m0, %0" : "=&s"(keep) : "v"(a), "s"(lb + L_JUNK) : "memory");
  }
  u32x4 ggw[2];
  if (w == 8) {
    const GAS u32x4* gp = (const GAS u32x4*)p.gw;
    ggw[0] = gp[lane]; ggw[1] = gp[64 + (lane & 7)];
  }
  const __amdgpu_buffer_rsrc_t rk = __builtin_amdgcn_make_buffer_rsrc((void*)p.K, 0, 0x10000000, 0x00020000);
  const __amdgpu_buffer_rsrc_t rv = __builtin_amdgcn_make_buffer_rsrc((void*)p.V, 0, 0x10000000, 0x00020000);
  const u32 kvoff = (u32)(((u32)(b * HKV + hkv) * SK + (SK - WIN) + s * 512 + w * 32) * (DH * 2));
  u64 tpub = 0;
  if (w < 4) {
    asm volatile("s_waitcnt vmcnt(0) lgkmcnt(0)" : "+s"(Eraw) :: "memory");
    const u32 tag = Eraw + 1u;
    if (lane == 0) lds_set(L, FL_WQ + w * 4, 1u);
    lds_wait4(L, FL_WQ);
    STAMP(2);
    const int cb = ii & 3, kq = ii >> 2;
    const u32 aoff = L_XA + (lane & 1) * 1152 + (144 * kq + 4 * g4) * 2;
    const u32 boff = L_WQ + (4 * g4 + q4) * 128 + p4 * 8 + w * 32;
    s16x4 fa[9], fb[9];
#pragma unroll
    for (int st = 0; st < 9; ++st) { fa[st] = lds_b64(L, aoff + st * 32); fb[st] = lds_tr(L, boff + st * 2048); }
    f32x4 acc = {0.f, 0.f, 0.f, 0.f};
#pragma unroll
    for (int st = 0; st < 9; ++st) acc = mfma16(fa[st], fb[st], acc);
    if (lane < 32) {
      const u32 o0 = WS_QP + (((hkv * 2 + g4) * 4 + kq) * 256 + 64 * cb + 16 * w + col) * 8;
      __builtin_amdgcn_raw_buffer_store_b64(u32x2{__float_as_uint(g4 ? acc[1] : acc[0]), tag}, rws, o0, 0, AUX_SC1);
    }
    tpub = __builtin_amdgcn_s_memrealtime();
    STAMP(3);
  }
  u32x4 kk[8], vv[8];
#pragma unroll
  for (int j = 0; j < 8; ++j) kk[j] = __builtin_amdgcn_raw_buffer_load_b128(rk, lane * 16, kvoff + j * 1024, AUX_NT);
#pragma unroll
  for (int j = 0; j < 8; ++j) vv[j] = __builtin_amdgcn_raw_buffer_load_b128(rv, lane * 16, kvoff + j * 1024, AUX_NT);
  __builtin_amdgcn_sched_barrier(0);
  if (lane == 0) __hip_atomic_fetch_add((LAS u32*)(L + L_FLAG + 4), 1u, __ATOMIC_RELAXED, __HIP_MEMORY_SCOPE_WORKGROUP);
  STAMP(1);
  asm volatile("s_waitcnt lgkmcnt(0)" : "+s"(Eraw) :: "memory");
  const u32 tag = Eraw + 1u;
  if (w < 4) {
    const u32 go = WS_QP + (((hkv * 2 + b) * 4 + (lane >> 5)) * 256 + 64 * w + 2 * (lane & 31)) * 8;
    delay_ticks(tpub, QDELAY);
    u32x4 qa, qb;
#pragma unroll 1
    for (u32 spin = 0;; ++spin) {
      asm volatile("" ::: "memory");
      qa = __builtin_amdgcn_raw_buffer_load_b128(rws, go, 0, AUX_SC1);
      qb = __builtin_amdgcn_raw_buffer_load_b128(rws, go + 2 * 2048, 0, AUX_SC1);
      if (__all((qa[1] == tag) & (qa[3] == tag) & (qb[1] == tag) & (qb[3] == tag)) || spin > SPIN_CAP) break;
      __builtin_amdgcn_s_sleep(8);
    }
    const float c0 = x32_sum(__uint_as_float(qa[0]) + __uint_as_float(qb[0])), c1 = x32_sum(__uint_as_float(qa[2]) + __uint_as_float(qb[2]));
    if (lane < 32) *(LAS u32*)(L + L_QS + w * 128 + lane * 4) = cvtpk(c0, c1);
    asm volatile("s_waitcnt lgkmcnt(0)" ::: "memory");
    if (lane == 0) lds_set(L, FL_Q + w * 4, 1u);
    STAMP(4);
  } else if (w < 8) {
    asm volatile("s_waitcnt vmcnt(16)" ::: "memory");
    if (lane == 0) lds_set(L, FL_W1 + (w - 4) * 4, 1u);
    if (wg < 216) {
      lds_wait4(L, FL_WQ);
      lds_wait4(L, FL_W1);
      const int cb = wg / 3, kt = wg - 3 * cb;
      const u32 aoff = L_XA + (lane & 1) * 1152 + (192 * kt + 4 * g4) * 2;
      const u32 boff = L_W1 + (4 * g4 + q4) * 128 + p4 * 8 + (w - 4) * 32;
      f32x4 acc = {0.f, 0.f, 0.f, 0.f};
#pragma unroll
      for (int hf = 0; hf < 2; ++hf) {
        s16x4 fa[6], fb[6];
#pragma unroll
        for (int st = 0; st < 6; ++st) { fa[st] = lds_b64(L, aoff + (hf * 6 + st) * 32); fb[st] = lds_tr(L, boff + (hf * 6 + st) * 2048); }
#pragma unroll
        for (int st = 0; st < 6; ++st) acc = mfma16(fa[st], fb[st], acc);
      }
      if (lane < 32) {
        const u32 o0 = WS_HP + ((kt * 2 + g4) * F + 64 * cb + 16 * (w - 4) + col) * 8;
        __builtin_amdgcn_raw_buffer_store_b64(u32x2{__float_as_uint(g4 ? acc[1] : acc[0]), tag}, rws, o0, 0, AUX_SC1);
      }
    }
  } else if (w == 8) {
    lds_wait4(L, FL_WQ);
    float s0 = 0.f, s1 = 0.f;
#pragma unroll
    for (int r = 0; r < 2; ++r) {
      const u32 xo = L_XA + (r ? 64 + (lane & 7) : lane) * 16;
      const u32x4 x0 = *(LAS u32x4*)(L + xo), x1 = *(LAS u32x4*)(L + xo + 1152);
      float t0 = 0.f, t1 = 0.f;
#pragma unroll
      for (int j = 0; j < 4; ++j) {
        const float glo = bf2f(ggw[r][j] & 0xffffu), ghi = __uint_as_float(ggw[r][j] & 0xffff0000u);
        t0 += rbf(bf2f(x0[j] & 0xffffu) * glo) + rbf(__uint_as_float(x0[j] & 0xffff0000u) * ghi);
        t1 += rbf(bf2f(x1[j] & 0xffffu) * glo) + rbf(__uint_as_float(x1[j] & 0xffff0000u) * ghi);
      }
      if (r == 0 || lane < 8) { s0 += t0; s1 += t1; }
    }
    s0 += dpp<XOR1>(s0); s0 += dpp<XOR2>(s0); s0 += dpp<ROR4>(s0); s0 += dpp<ROR8>(s0); s0 = x32_sum(x16_sum(s0));
    s1 += dpp<XOR1>(s1); s1 += dpp<XOR2>(s1); s1 += dpp<ROR4>(s1); s1 += dpp<ROR8>(s1); s1 = x32_sum(x16_sum(s1));
    const float g0 = rbf(1.f / (1.f + __expf(-rbf(s0)))), g1 = rbf(1.f / (1.f + __expf(-rbf(s1))));
    if (lane == 0) *(LAS f32x4*)(L + L_G) = f32x4{g0, g1, rbf(1.f - g0), rbf(1.f - g1)};
  }

  lds_wait4(L, FL_Q);
  const u32x4 q0 = *(LAS u32x4*)(L + L_QS + col * 16), q1 = *(LAS u32x4*)(L + L_QS + 256 + col * 16);
  STAMP(5);
  float x[16];
#pragma unroll
  for (int i = 0; i < 8; ++i) {
    float d0 = 0.f, d1 = 0.f;
#pragma unroll
    for (int j = 0; j < 4; ++j) { d0 = dot2(kk[i][j], q0[j], d0); d1 = dot2(kk[i][j], q1[j], d1); }
    x[2 * i] = d0; x[2 * i + 1] = d1;
  }
#define TR_STEP(HALF, CTRL, BIT) _Pragma("unroll") for (int h = 0; h < (HALF); ++h) { \
    const float lo_ = x[h], hi_ = x[h + (HALF)]; const float t1_ = lo_ + dpp<CTRL>(lo_), t2_ = hi_ + dpp<CTRL>(hi_); \
    x[h] = (col & (BIT)) ? t2_ : t1_; }
  TR_STEP(8, XOR8, 8) TR_STEP(4, XOR7, 4) TR_STEP(2, XOR2, 2) TR_STEP(1, XOR1, 1)
#undef TR_STEP
  const float sc = rbf(rbf(x[0]) * 0.08838834764831845f) * LOG2E;
  float mx = sc;
  mx = fmaxf(mx, dpp<XOR2>(mx)); mx = fmaxf(mx, dpp<ROR4>(mx)); mx = fmaxf(mx, dpp<ROR8>(mx)); mx = x32_max(x16_max(mx));
  const float pr = __builtin_amdgcn_exp2f(sc - mx);
  STAMP(6);
  f32x2 a0[4], a1[4];
#pragma unroll
  for (int j = 0; j < 4; ++j) { a0[j] = f32x2{0.f, 0.f}; a1[j] = f32x2{0.f, 0.f}; }
#define PV(i) { const float p0_ = dpp<BCAST + 2 * (i)>(pr), p1_ = dpp<BCAST + 2 * (i) + 1>(pr); \
    _Pragma("unroll") for (int j = 0; j < 4; ++j) { const f32x2 vf = {__uint_as_float(vv[i][j] << 16), __uint_as_float(vv[i][j] & 0xffff0000u)}; \
      a0[j] = __builtin_elementwise_fma(vf, f32x2{p0_, p0_}, a0[j]); a1[j] = __builtin_elementwise_fma(vf, f32x2{p1_, p1_}, a1[j]); } }
  float ls = pr;
  ls += dpp<XOR2>(ls); ls += dpp<ROR4>(ls); ls += dpp<ROR8>(ls); ls = x32_sum(x16_sum(ls));
  if (lane < 2) { *(LAS float*)(L + L_MM + lane * 64 + w * 4) = mx; *(LAS float*)(L + L_ML + lane * 64 + w * 4) = ls; }
  PV(0) PV(1) PV(2) PV(3) PV(4) PV(5) PV(6) PV(7)
#undef PV
  float y[8];
#pragma unroll
  for (int k = 0; k < 8; ++k) {
    auto r = __builtin_amdgcn_permlane32_swap(__float_as_uint(a0[k >> 1][k & 1]), __float_as_uint(a1[k >> 1][k & 1]), false, false);
    y[k] = __uint_as_float(r[0]) + __uint_as_float(r[1]);
  }
  f32x4 z;
#pragma unroll
  for (int k = 0; k < 4; ++k) {
    auto r = __builtin_amdgcn_permlane16_swap(__float_as_uint(y[k]), __float_as_uint(y[k + 4]), false, false);
    z[k] = __uint_as_float(r[0]) + __uint_as_float(r[1]);
  }
  *(LAS f32x4*)(L + L_MRG + w * 1024 + ((g4 >> 1) * 128 + 8 * col + 4 * (g4 & 1)) * 4) = z;
  asm volatile("s_waitcnt lgkmcnt(0)" ::: "memory");
  if (lane == 0) __hip_atomic_fetch_add((LAS u32*)(L + L_FLAG + 8), 1u, __ATOMIC_RELAXED, __HIP_MEMORY_SCOPE_WORKGROUP);
  STAMP(7);
  if (w == 5) {
    const int cc = lane < 36 ? lane : 35;
    const u32 ho = WS_HP + (36 * u + cc) * 8;
    u32x2 hg[6];
    const float bb = bf2f(p.b1[36 * u + cc]);
#pragma unroll 1
    for (u32 spin = 0;; ++spin) {
      asm volatile("" ::: "memory");
#pragma unroll
      for (int j = 0; j < 6; ++j) hg[j] = __builtin_amdgcn_raw_buffer_load_b64(rws, ho + j * F * 8, 0, AUX_SC1);
      bool ok = true;
#pragma unroll
      for (int j = 0; j < 6; ++j) ok &= hg[j][1] == tag;
      if (__all(ok) || spin > SPIN_CAP) break;
      __builtin_amdgcn_s_sleep(16);
    }
#pragma unroll
    for (int bb2 = 0; bb2 < 2; ++bb2) {
      float hs = __uint_as_float(hg[bb2][0]) + __uint_as_float(hg[2 + bb2][0]) + __uint_as_float(hg[4 + bb2][0]);
      hs = rbf(rbf(hs) + bb);
      hs = hs > 0.f ? hs : 0.f;
      const u32 hv = lane < 36 ? (cvtpk(hs * hs, 0.f) & 0xffffu) : 0u;
      if (lane < 48) *(LAS u16*)(L + L_AH + bb2 * 96 + lane * 2) = (u16)hv;
    }
  }
  const int fb_ = wg / 72, c72 = wg - 72 * fb_, fcg = c72 / 36, ch = c72 - 36 * fcg;
  u32 bias2 = 0;
  if (w == 0 && wg < 144 && lane < 4) bias2 = *(const GAS u32*)(p.b2 + fcg * 288 + ch * 8 + 2 * lane);
  if (w >= 5 && w < 9) {
#pragma unroll 1
    for (u32 spin = 0; spin < (1u << 22); ++spin) {
      if (lds_flag(L, L_FLAG + 4) == 16u) break;
      __builtin_amdgcn_s_sleep(1);
    }
    load_wo_w2<3>(p, lb, lane, w - 5, 4, hc, rb, cg, u);
    STAMP(3);
    asm volatile("s_waitcnt vmcnt(0) lgkmcnt(0)" ::: "memory");
    if (lane == 0) lds_set(L, FL_WD + (w - 5) * 4, 1u);
    STAMP(10);
  }
  if (w < 4) {
    const int h = w >> 1, d = tid & 127;
    const u32 rec = WS_P1 + (((hkv * 8 + h * 4 + (d >> 5)) * 16) + b * 8 + s) * 288;
#pragma unroll 1
    for (int pass = DRY ? 0 : 1; pass < 2; ++pass) {
      asm volatile("" : "+s"(pass));
      if (pass) {
#pragma unroll 1
        for (u32 spin = 0; spin < (1u << 22); ++spin) {
          if (lds_flag(L, L_FLAG + 8) == 16u) break;
        }
        asm volatile("" ::: "memory");
        if (lane < 4) __builtin_amdgcn_raw_buffer_store_b32(tag, rws, WS_F1 + ((hkv * 16 + w * 4 + lane) * 32 + ii) * 4, 0, AUX_SC1);
        STAMP(8);
      }
      float ov[16];
#pragma unroll
      for (int j = 0; j < 16; ++j) ov[j] = *(LAS float*)(L + L_MRG + j * 1024 + tid * 4);
      const float mj = *(LAS float*)(L + L_MM + h * 64 + col * 4), lj = *(LAS float*)(L + L_ML + h * 64 + col * 4);
      float M = mj;
      M = fmaxf(M, dpp<XOR1>(M)); M = fmaxf(M, dpp<XOR2>(M)); M = fmaxf(M, dpp<ROR4>(M)); M = fmaxf(M, dpp<ROR8>(M));
      const float e = __builtin_amdgcn_exp2f(mj - M);
      float Ls = e * lj;
      Ls += dpp<XOR1>(Ls); Ls += dpp<XOR2>(Ls); Ls += dpp<ROR4>(Ls); Ls += dpp<ROR8>(Ls);
      float o = 0.f;
#pragma unroll
      for (int j = 0; j < 16; ++j) o = fmaf(__uint_as_float(__builtin_amdgcn_readlane(__float_as_uint(e), j)), ov[j], o);
      asm volatile("" :: "v"(o), "v"(Ls));
      if (pass) {
        __builtin_amdgcn_raw_buffer_store_b64(u32x2{__float_as_uint(o), tag}, rws, rec + (d & 31) * 8, 0, AUX_SC1);
        if ((d & 31) < 2) __builtin_amdgcn_raw_buffer_store_b64(u32x2{__float_as_uint((d & 1) ? Ls : M), tag}, rws, rec + 256 + (d & 1) * 8, 0, AUX_SC1);
        STAMP(9);
      }
    }
  } else if (w == 4) {
    const int gb = lane >> 5, gs = (lane >> 2) & 7, dq = lane & 3;
    const u32 rec = WS_P1 + (((hc * 8 + rb) * 16) + gb * 8 + gs) * 288;
#pragma unroll 1
    for (int pass = DRY ? 0 : 1; pass < 2; ++pass) {
      asm volatile("" : "+s"(pass));
      u32x4 og[4], ml;
      if (pass) {
        u32 pf[4];
#pragma unroll 1
        for (u32 spin = 0; spin < (1u << 22); ++spin) { if (lds_flag(L, L_FLAG + 8) == 16u) break; __builtin_amdgcn_s_sleep(1); }
        wait_hint(p.ws, WS_F1 + ((hc * 16 + ii) * 32 + (lane & 15)) * 4, tag, pf);
#define SEND_HINT2() if (rb == 0) { \
          __builtin_amdgcn_raw_buffer_store_b32(tag, rws, WS_F2 + (((lane & 1) * 72 + cg * 36 + (lane >> 1)) * 32 + hc) * 4, 0, AUX_SC1); \
          if (lane < 8) __builtin_amdgcn_raw_buffer_store_b32(tag, rws, WS_F2 + (((lane & 1) * 72 + cg * 36 + 32 + (lane >> 1)) * 32 + hc) * 4, 0, AUX_SC1); }
        SEND_HINT2()
        STAMP(15);
#pragma unroll 1
        for (u32 spin = 0;; ++spin) {
          asm volatile("" ::: "memory");
#pragma unroll
          for (int j = 0; j < 4; ++j) og[j] = __builtin_amdgcn_raw_buffer_load_b128(rws, rec + dq * 64 + j * 16, 0, AUX_SC1);
          ml = __builtin_amdgcn_raw_buffer_load_b128(rws, rec + 256, 0, AUX_SC1);
          bool ok = (ml[1] == tag) & (ml[3] == tag);
#pragma unroll
          for (int j = 0; j < 4; ++j) ok &= (og[j][1] == tag) & (og[j][3] == tag);
          if (__all(ok) || spin > SPIN_CAP) break;
          __builtin_amdgcn_s_sleep(RSLEEP);
        }
        KEEP_POLLS(pf);
      } else {
#pragma unroll
        for (int j = 0; j < 4; ++j) og[j] = q0;
        ml = q1;
      }
      const float m = __uint_as_float(ml[0]);
      float M = m;
      M = fmaxf(M, dpp<ROR4>(M)); M = fmaxf(M, dpp<ROR8>(M)); M = x16_max(M);
      const float e = __builtin_amdgcn_exp2f(m - M);
      float Ls = e * __uint_as_float(ml[2]);
      Ls += dpp<ROR4>(Ls); Ls += dpp<ROR8>(Ls); Ls = x16_sum(Ls);
      const float inv = 1.f / Ls;
      float ov[8];
#pragma unroll
      for (int k = 0; k < 8; ++k) {
        float t = e * __uint_as_float(og[k >> 1][(k & 1) * 2]);
        t += dpp<ROR4>(t); t += dpp<ROR8>(t); t = x16_sum(t);
        ov[k] = t * inv;
      }
      if (gs == 0) *(LAS u32x4*)(L + L_AO + gb * 64 + dq * 16) = u32x4{cvtpk(ov[0], ov[1]), cvtpk(ov[2], ov[3]), cvtpk(ov[4], ov[5]), cvtpk(ov[6], ov[7])};
      if (pass) STAMP(10);
    }
  }
  if (w < 10 && w != 4) {
    const int mw = w < 4 ? w : w - 1;
    const u32 ao = L_AO + (lane & 1) * 64 + 4 * g4 * 2, ah = L_AH + (lane & 1) * 96 + 4 * g4 * 2;
    const int rr = 4 * g4 + q4, tb = g4 & 1, tt = g4 >> 1;
    lds_wait4(L, FL_WD);
    f32x4 df[2];
    s16x4 bo[2][2];
#pragma unroll
    for (int t = 0; t < 2; ++t) df[t] = f32x4{0.f, 0.f, 0.f, 0.f};
#pragma unroll
    for (int st = 0; st < 3; ++st) {
      const s16x4 a = lds_b64(L, ah + st * 32);
      const int row = min(16 * st + rr, 35);
#pragma unroll
      for (int t = 0; t < 2; ++t) df[t] = mfma16(a, lds_tr(L, L_W2 + row * 576 + (2 * mw + t) * 32 + p4 * 8), df[t]);
    }
#pragma unroll
    for (int st = 0; st < 2; ++st)
#pragma unroll
      for (int t = 0; t < 2; ++t) bo[st][t] = lds_tr(L, L_WO + (16 * st + rr) * 576 + (2 * mw + t) * 32 + p4 * 8);
    const f32x4 gt = *(const LAS f32x4*)(L + L_G);
    const float ga = tb ? gt[1] : gt[0];
    const float vf = (tb ? gt[3] : gt[2]) * (tt ? (tb ? df[1][1] : df[1][0]) : (tb ? df[0][1] : df[0][0]));
    const int n = (2 * mw + tt) * 16 + col;
    const u32 off = WS_P2 + ((((cg * 2 + tb) * 36 + (n >> 3)) * 128) + u) * 64 + (n & 7) * 8;
#pragma unroll 1
    for (int pass = DRY ? 0 : 1; pass < 2; ++pass) {
      asm volatile("" : "+s"(pass));
      if (pass) { asm volatile("s_waitcnt lgkmcnt(0)\n\ts_barrier" ::: "memory"); STAMP(11); }
      f32x4 da[2];
#pragma unroll
      for (int t = 0; t < 2; ++t) da[t] = f32x4{0.f, 0.f, 0.f, 0.f};
#pragma unroll
      for (int st = 0; st < 2; ++st) {
        const s16x4 a = lds_b64(L, ao + st * 32);
#pragma unroll
        for (int t = 0; t < 2; ++t) da[t] = mfma16(a, bo[st][t], da[t]);
      }
      const float val = ga * (tt ? (tb ? da[1][1] : da[1][0]) : (tb ? da[0][1] : da[0][0])) + vf;
      asm volatile("" :: "v"(val));
      if (pass) { __builtin_amdgcn_raw_buffer_store_b64(u32x2{__float_as_uint(val), tag}, rws, off, 0, AUX_SC1); STAMP(12); }
    }
  } else {
    asm volatile("s_waitcnt lgkmcnt(0)\n\ts_barrier" ::: "memory");
  }
  if (wg >= 144 || w != 0) { STAMP_DUMP(); return; }
  {
    const u32 off = WS_P2 + (((fcg * 2 + fb_) * 36 + ch) * 128) * 64 + lane * 16;
    u32x4 pg[8];
    u32 pf[4];
    wait_hint(p.ws, WS_F2 + (wg * 32 + (lane & 15)) * 4, tag, pf);
    STAMP(14);
    if (HD2) delay_ticks(__builtin_amdgcn_s_memrealtime(), HD2);
#pragma unroll 1
    for (u32 spin = 0;; ++spin) {
      asm volatile("" ::: "memory");
#pragma unroll
      for (int j = 0; j < 8; ++j) pg[j] = __builtin_amdgcn_raw_buffer_load_b128(rws, off + j * 1024, 0, AUX_SC1);
      bool ok = true;
#pragma unroll
      for (int j = 0; j < 8; ++j) ok &= (pg[j][1] == tag) & (pg[j][3] == tag);
      if (__all(ok) || spin > SPIN_CAP) break;
      __builtin_amdgcn_s_sleep(RSLEEP2);
    }
    KEEP_POLLS(pf);
    float t0 = 0.f, t1 = 0.f;
#pragma unroll
    for (int j = 0; j < 8; ++j) { t0 += __uint_as_float(pg[j][0]); t1 += __uint_as_float(pg[j][2]); }
    t0 += dpp<ROR4>(t0); t0 += dpp<ROR8>(t0); t0 = x32_sum(x16_sum(t0));
    t1 += dpp<ROR4>(t1); t1 += dpp<ROR8>(t1); t1 = x32_sum(x16_sum(t1));
    const float og_ = ((const LAS float*)(L + L_G))[2 + fb_];
    t0 += og_ * bf2f(bias2 & 0xffffu); t1 += og_ * __uint_as_float(bias2 & 0xffff0000u);
    if (lane < 4) *(GAS u32*)(p.out + fb_ * D + fcg * 288 + ch * 8 + 2 * lane) = cvtpk(t0, t1);
    if (wg == 0 && lane == 0) __hip_atomic_store((GAS u32*)(p.ws + WS_E), tag, __ATOMIC_RELAXED, __HIP_MEMORY_SCOPE_AGENT);
    STAMP(13);
    STAMP_DUMP();
  }
}

extern "C" void kernel_launch(void* const* d_in, const int* in_sizes, int n_in, void* d_out, int out_size, void* d_ws, size_t ws_size,
                              hipStream_t stream) {
  Params p;
  p.x = (const u16*)d_in[0]; p.K = (const u16*)d_in[1]; p.V = (const u16*)d_in[2]; p.wq = (const u16*)d_in[3]; p.wo = (const u16*)d_in[4];
  p.w1 = (const u16*)d_in[6]; p.b1 = (const u16*)d_in[7]; p.w2 = (const u16*)d_in[8]; p.b2 = (const u16*)d_in[9]; p.gw = (const u16*)d_in[10];
  p.out = (u16*)d_out; p.ws = (unsigned char*)d_ws;
  (void)hipFuncSetAttribute((const void*)fused_kernel, hipFuncAttributeMaxDynamicSharedMemorySize, L_END);
  hipLaunchKernelGGL(fused_kernel, dim3(NWG), dim3(NTHR), L_END, stream, p);
}
```

```cpp
#include <hip/hip_runtime.h>
#include <stdint.h>

typedef unsigned short u16;
typedef unsigned int u32;
typedef unsigned long long u64;
typedef __attribute__((ext_vector_type(4))) unsigned u32x4;
typedef __attribute__((ext_vector_type(2))) unsigned u32x2;
typedef __attribute__((ext_vector_type(4))) float f32x4;
typedef __attribute__((ext_vector_type(2))) float f32x2;
typedef __attribute__((ext_vector_type(4))) short s16x4;
typedef __attribute__((ext_vector_type(2))) __bf16 bf16x2;
#define GAS __attribute__((address_space(1)))
#define LAS __attribute__((address_space(3)))

constexpr int D = 576, HKV = 16, SK = 32768, DH = 128, WIN = 4096, F = 4608;
constexpr int NWG = 256, NTHR = 1024;
constexpr int NREP = 18;
constexpr u32 QDELAY = 220;
constexpr u32 SPIN_CAP = 1u << 16;
constexpr u32 LDS_CAP = 1u << 22;

constexpr u32 WS_E = 0;
constexpr u32 WS_QP = 256;
constexpr u32 WS_HP = WS_QP + 16 * 2 * 4 * 256 * 8;
constexpr u32 WS_P1 = WS_HP + 3 * 2 * F * 8;
constexpr u32 WS_P2 = WS_P1 + 16 * 8 * 16 * 288;
constexpr u32 WS_F1 = WS_P2 + 2 * 2 * 36 * 128 * 64;
constexpr u32 WS_F2 = WS_F1 + 16 * 16 * 128;
constexpr u32 WS_END = WS_F2 + 2 * NREP * 128;

constexpr u32 L_X = 0, L_WQ = 3072, L_W1 = L_WQ + 18432, L_WO = L_W1 + 24576, L_W2 = L_WO + 18432;
constexpr u32 L_MRG = L_W2 + 21504;
constexpr u32 L_MM = L_MRG + 16384;
constexpr u32 L_ML = L_MM + 128;
constexpr u32 L_QS = L_ML + 128;
constexpr u32 L_AO = L_QS + 512;
constexpr u32 L_AH = L_AO + 128;
constexpr u32 L_G = L_AH + 192;
constexpr u32 L_FLAG = L_G + 16;
constexpr u32 C_ISSUED = L_FLAG, C_DONE = L_FLAG + 4;
constexpr u32 FL_WQ = L_FLAG + 16, FL_W1 = L_FLAG + 32, FL_Q = L_FLAG + 48, FL_WD = L_FLAG + 64;
constexpr u32 L_JUNK = L_FLAG + 128;
constexpr u32 L_END = L_JUNK + 256;

struct Params {
  const u16* x; const u16* K; const u16* V; const u16* wq; const u16* wo; const u16* w1;
  const u16* b1; const u16* w2; const u16* b2; const u16* gw; u16* out; unsigned char* ws;
};

constexpr int AUX_NT = 2, AUX_SC1 = 16;
constexpr float LOG2E = 1.4426950408889634f;

__device__ __forceinline__ float bf_lo(u32 v) { return __uint_as_float(v << 16); }
__device__ __forceinline__ float bf_hi(u32 v) { return __uint_as_float(v & 0xffff0000u); }
__device__ __forceinline__ u32 cvtpk(float lo, float hi) {
  u32 r; asm("v_cvt_pk_bf16_f32 %0, %1, %2" : "=v"(r) : "v"(lo), "v"(hi)); return r;
}
__device__ __forceinline__ float rbf(float f) { return __uint_as_float(cvtpk(f, f) << 16); }
__device__ __forceinline__ float dot2(u32 a, u32 b, float acc) {
  return __builtin_amdgcn_fdot2_f32_bf16(__builtin_bit_cast(bf16x2, a), __builtin_bit_cast(bf16x2, b), acc, false);
}
template <int CTRL> __device__ __forceinline__ float dpp(float x) {
  return __builtin_bit_cast(float, __builtin_amdgcn_mov_dpp(__builtin_bit_cast(int, x), CTRL, 0xf, 0xf, true));
}
constexpr int XOR1 = 0xB1, XOR2 = 0x4E, XOR7 = 0x141, ROR4 = 0x124, ROR8 = 0x128, BCAST = 0x150;
__device__ __forceinline__ float x16_max(float x) {
  auto s = __builtin_amdgcn_permlane16_swap(__float_as_uint(x), __float_as_uint(x), false, false);
  return fmaxf(__uint_as_float(s[0]), __uint_as_float(s[1]));
}
__device__ __forceinline__ float x32_max(float x) {
  auto s = __builtin_amdgcn_permlane32_swap(__float_as_uint(x), __float_as_uint(x), false, false);
  return fmaxf(__uint_as_float(s[0]), __uint_as_float(s[1]));
}
__device__ __forceinline__ float x16_sum(float x) {
  auto s = __builtin_amdgcn_permlane16_swap(__float_as_uint(x), __float_as_uint(x), false, false);
  return __uint_as_float(s[0]) + __uint_as_float(s[1]);
}
__device__ __forceinline__ float x32_sum(float x) {
  auto s = __builtin_amdgcn_permlane32_swap(__float_as_uint(x), __float_as_uint(x), false, false);
  return __uint_as_float(s[0]) + __uint_as_float(s[1]);
}
template <bool NT> __device__ __forceinline__ void glds16(const void* sbase, u32 voff, u32 lds_dst) {
  unsigned keep;
  if (NT)
    asm volatile("s_mov_b32 %0, m0\n\ts_mov_b32 m0, %3\n\ts_nop 0\n\tglobal_load_lds_dwordx4 %1, %2 nt\n\ts_mov_b32 m0, %0" : "=&s"(keep) : "v"(voff), "s"(sbase), "s"(lds_dst) : "memory");
  else
    asm volatile("s_mov_b32 %0, m0\n\ts_mov_b32 m0, %3\n\ts_nop 0\n\tglobal_load_lds_dwordx4 %1, %2\n\ts_mov_b32 m0, %0" : "=&s"(keep) : "v"(voff), "s"(sbase), "s"(lds_dst) : "memory");
}
__device__ __forceinline__ f32x4 mfma16(s16x4 a, s16x4 b, f32x4 c) { return __builtin_amdgcn_mfma_f32_16x16x16bf16_1k(a, b, c, 0, 0, 0); }
__device__ __forceinline__ s16x4 lds_tr(LAS unsigned char* L, u32 off) { return __builtin_amdgcn_ds_read_tr16_b64_v4i16((LAS s16x4*)(L + off)); }
__device__ __forceinline__ s16x4 lds_b64(LAS unsigned char* L, u32 off) { return *(LAS s16x4*)(L + off); }
__device__ __forceinline__ u32 lds_get(LAS unsigned char* L, u32 off) { return __hip_atomic_load((LAS u32*)(L + off), __ATOMIC_RELAXED, __HIP_MEMORY_SCOPE_WORKGROUP); }
__device__ __forceinline__ void lds_set(LAS unsigned char* L, u32 off, u32 v) { __hip_atomic_store((LAS u32*)(L + off), v, __ATOMIC_RELAXED, __HIP_MEMORY_SCOPE_WORKGROUP); }
__device__ __forceinline__ void lds_inc(LAS unsigned char* L, u32 off) { __hip_atomic_fetch_add((LAS u32*)(L + off), 1u, __ATOMIC_RELAXED, __HIP_MEMORY_SCOPE_WORKGROUP); }
__device__ __forceinline__ void lds_wait4(LAS unsigned char* L, u32 off) {
#pragma unroll 1
  for (u32 spin = 0; spin < LDS_CAP; ++spin) {
    if (__all(lds_get(L, off + (__lane_id() & 3) * 4) != 0u)) break;
    __builtin_amdgcn_s_sleep(1);
  }
  asm volatile("" ::: "memory");
}
template <bool SLEEP> __device__ __forceinline__ void lds_wait16(LAS unsigned char* L, u32 off) {
#pragma unroll 1
  for (u32 spin = 0; spin < LDS_CAP; ++spin) {
    if (lds_get(L, off) == 16u) break;
    if (SLEEP) __builtin_amdgcn_s_sleep(1);
  }
  asm volatile("" ::: "memory");
}
__device__ __forceinline__ void wait_hint(__amdgpu_buffer_rsrc_t r, u32 off, u32 tag) {
#pragma unroll 1
  for (u32 spin = 0; spin < SPIN_CAP; ++spin) {
    asm volatile("" ::: "memory");
    if (__all(__builtin_amdgcn_raw_buffer_load_b32(r, off, 0, AUX_SC1) == tag)) break;
  }
}

__global__ void __launch_bounds__(NTHR, 4) fused_kernel(Params p) {
  extern __shared__ __attribute__((aligned(16))) unsigned char smem[];
  LAS unsigned char* L = (LAS unsigned char*)smem;
  const u32 lb = (u32)(uintptr_t)L;
  const int tid = threadIdx.x, lane = tid & 63, w = __builtin_amdgcn_readfirstlane(tid >> 6);
  const int wg = blockIdx.x;
  const int idx = wg >> 3, hkv = (wg & 7) * 2 + (idx >> 4), ii = idx & 15, b = ii >> 3, s = ii & 7;
  const int hc = (hkv + 2) & 15, rb = ii >> 1, cg = ii & 1, u = hc * 8 + rb;
  const int fb = wg / 72, c72 = wg - 72 * fb, fcg = c72 / 36, ch = c72 - 36 * fcg;
  const __amdgpu_buffer_rsrc_t rws = __builtin_amdgcn_make_buffer_rsrc((void*)p.ws, 0, WS_END, 0x00020000);
  const int g4 = lane >> 4, col = lane & 15, q4 = col >> 2, p4 = lane & 3;

  if (w < 4) {
    const int cb = ii & 3, kq = ii >> 2;
    const u32 voff = (lane >> 3) * 8192 + (lane & 7) * 16;
    const char* base = (const char*)p.wq + (size_t)(144 * kq + 8 * w) * 8192 + (hkv * 256 + 64 * cb) * 2;
    if (w < 2) glds16<false>(p.x, w * 1024 + lane * 16, lb + L_X + w * 1024);
    else if (w == 2) { if (lane < 16) glds16<false>(p.x, 2048 + lane * 16, lb + L_X + 2048); }
#pragma unroll 1
    for (int j = w; j < 18; j += 4) { glds16<true>(base, voff, lb + L_WQ + j * 1024); base += 32 * 8192; }
  } else if (w == 15) {
    if (lane < 32) lds_set(L, L_FLAG + lane * 4, 0u);
    asm volatile("s_waitcnt lgkmcnt(0)" ::: "memory");
  }
  __builtin_amdgcn_s_barrier();
  u32 epoch;
  asm volatile("s_load_dword %0, %1, 0x0" : "=s"(epoch) : "s"(p.ws) : "memory");

  if (w >= 4 && w < 8 && wg < 216) {
    const int cb = wg / 3, kt = wg - 3 * cb;
    const u32 voff = (lane >> 3) * 9216 + (lane & 7) * 16;
    const char* base = (const char*)p.w1 + (size_t)(192 * kt + 8 * (w - 4)) * 9216 + cb * 128;
#pragma unroll 1
    for (int j = w - 4; j < 24; j += 4) { glds16<true>(base, voff, lb + L_W1 + j * 1024); base += 32 * 9216; }
  }
  if (w == 14) {
    u32 o = WS_HP + 36 * u * 8;
    if (lane < 36) o = WS_P2 + (((cg * 2 + (lane >= 18)) * 36 + 2 * (lane >= 18 ? lane - 18 : lane)) * 128 + u) * 64;
    else if (lane < 44) o = WS_P2 + (((fcg * 2 + (fb & 1)) * 36 + ch) * 128) * 64 + (lane - 36) * 1024;
    else if (lane == 44) o = WS_F1 + (hc * 16 + ii) * 128;
    else if (lane == 45) o = WS_F2 + (fcg * NREP + wg % NREP) * 128;
    else if (lane == 46) o = WS_P1 + ((hc * 8 + rb) * 16) * 288;
    else if (lane == 47) o = WS_P1 + ((hkv * 8) * 16 + b * 8 + s) * 288;
    else if (lane == 48) o = WS_P1 + ((hkv * 8 + 7) * 16 + b * 8 + s) * 288;
    else if (lane == 49) o = WS_F1 + hkv * 16 * 128;
    else if (lane == 50) o = WS_F2 + cg * NREP * 128;
    const unsigned char* a = p.ws + o;
    if (lane >= 52 && lane < 56) a = (const unsigned char*)p.wo + (size_t)(hc * 256 + 32 * rb + (lane - 52) * 10) * 1152 + cg * 576;
    else if (lane >= 56 && lane < 60) a = (const unsigned char*)p.w2 + (size_t)(36 * u + (lane - 56) * 11) * 1152 + cg * 576;
    else if (lane == 60) a = (const unsigned char*)(p.b1 + 36 * u);
    else if (lane == 61) a = (const unsigned char*)(p.b2 + fcg * 288 + ch * 8);
    else if (lane == 62) a = (const unsigned char*)(p.out + (fb & 1) * D + fcg * 288 + ch * 8);
    unsigned keep;
    asm volatile("s_mov_b32 %0, m0\n\ts_mov_b32 m0, %2\n\ts_nop 0\n\tglobal_load_lds_dword %1, off sc1\n\ts_mov_b32 m0, %0" : "=&s"(keep) : "v"(a), "s"(lb + L_JUNK) : "memory");
  }
  u32x4 ggw[2];
  if (w == 8) {
    const GAS u32x4* gp = (const GAS u32x4*)p.gw;
    ggw[0] = gp[lane]; ggw[1] = gp[64 + (lane & 7)];
  }
  const __amdgpu_buffer_rsrc_t rk = __builtin_amdgcn_make_buffer_rsrc((void*)p.K, 0, 0x10000000, 0x00020000);
  const __amdgpu_buffer_rsrc_t rv = __builtin_amdgcn_make_buffer_rsrc((void*)p.V, 0, 0x10000000, 0x00020000);
  const u32 kvoff = (u32)(((u32)(b * HKV + hkv) * SK + (SK - WIN) + s * 512 + w * 32) * (DH * 2));

  u64 tpub = 0;
  if (w < 4) {
    asm volatile("s_waitcnt vmcnt(0) lgkmcnt(0)" : "+s"(epoch) :: "memory");
    if (lane == 0) lds_set(L, FL_WQ + w * 4, 1u);
    lds_wait4(L, FL_WQ);
    const int cb = ii & 3, kq = ii >> 2;
    const u32 aoff = L_X + (lane & 1) * 1152 + (144 * kq + 4 * g4) * 2;
    const u32 boff = L_WQ + (4 * g4 + q4) * 128 + p4 * 8 + w * 32;
    s16x4 fa[9], fb_[9];
#pragma unroll
    for (int st = 0; st < 9; ++st) { fa[st] = lds_b64(L, aoff + st * 32); fb_[st] = lds_tr(L, boff + st * 2048); }
    f32x4 acc = {0.f, 0.f, 0.f, 0.f};
#pragma unroll
    for (int st = 0; st < 9; ++st) acc = mfma16(fa[st], fb_[st], acc);
    if (lane < 32) {
      const u32 o0 = WS_QP + (((hkv * 2 + g4) * 4 + kq) * 256 + 64 * cb + 16 * w + col) * 8;
      __builtin_amdgcn_raw_buffer_store_b64(u32x2{__float_as_uint(g4 ? acc[1] : acc[0]), epoch + 1u}, rws, o0, 0, AUX_SC1);
    }
    tpub = __builtin_amdgcn_s_memrealtime();
  }

  u32x4 kk[8], vv[8];
#pragma unroll
  for (int j = 0; j < 8; ++j) kk[j] = __builtin_amdgcn_raw_buffer_load_b128(rk, lane * 16, kvoff + j * 1024, AUX_NT);
#pragma unroll
  for (int j = 0; j < 8; ++j) vv[j] = __builtin_amdgcn_raw_buffer_load_b128(rv, lane * 16, kvoff + j * 1024, AUX_NT);
  __builtin_amdgcn_sched_barrier(0);
  if (lane == 0) lds_inc(L, C_ISSUED);
  asm volatile("s_waitcnt lgkmcnt(0)" : "+s"(epoch) :: "memory");
  const u32 tag = epoch + 1u;

  if (w < 4) {
    const u32 go = WS_QP + (((hkv * 2 + b) * 4 + (lane >> 5)) * 256 + 64 * w + 2 * (lane & 31)) * 8;
    while (__builtin_amdgcn_s_memrealtime() - tpub < QDELAY) __builtin_amdgcn_s_sleep(1);
    u32x4 qa, qb;
#pragma unroll 1
    for (u32 spin = 0;; ++spin) {
      asm volatile("" ::: "memory");
      qa = __builtin_amdgcn_raw_buffer_load_b128(rws, go, 0, AUX_SC1);
      qb = __builtin_amdgcn_raw_buffer_load_b128(rws, go + 2 * 2048, 0, AUX_SC1);
      if (__all((qa[1] == tag) & (qa[3] == tag) & (qb[1] == tag) & (qb[3] == tag)) || spin > SPIN_CAP) break;
      __builtin_amdgcn_s_sleep(8);
    }
    const float c0 = x32_sum(__uint_as_float(qa[0]) + __uint_as_float(qb[0])), c1 = x32_sum(__uint_as_float(qa[2]) + __uint_as_float(qb[2]));
    if (lane < 32) *(LAS u32*)(L + L_QS + w * 128 + lane * 4) = cvtpk(c0, c1);
    asm volatile("s_waitcnt lgkmcnt(0)" ::: "memory");
    if (lane == 0) lds_set(L, FL_Q + w * 4, 1u);
  } else if (w < 8) {
    asm volatile("s_waitcnt vmcnt(16)" ::: "memory");
    if (lane == 0) lds_set(L, FL_W1 + (w - 4) * 4, 1u);
    if (wg < 216) {
      lds_wait4(L, FL_WQ);
      lds_wait4(L, FL_W1);
      const int cb = wg / 3, kt = wg - 3 * cb;
      const u32 aoff = L_X + (lane & 1) * 1152 + (192 * kt + 4 * g4) * 2;
      const u32 boff = L_W1 + (4 * g4 + q4) * 128 + p4 * 8 + (w - 4) * 32;
      f32x4 acc = {0.f, 0.f, 0.f, 0.f};
#pragma unroll
      for (int hf = 0; hf < 2; ++hf) {
        s16x4 fa[6], fb_[6];
#pragma unroll
        for (int st = 0; st < 6; ++st) { fa[st] = lds_b64(L, aoff + (hf * 6 + st) * 32); fb_[st] = lds_tr(L, boff + (hf * 6 + st) * 2048); }
#pragma unroll
        for (int st = 0; st < 6; ++st) acc = mfma16(fa[st], fb_[st], acc);
      }
      if (lane < 32) {
        const u32 o0 = WS_HP + ((kt * 2 + g4) * F + 64 * cb + 16 * (w - 4) + col) * 8;
        __builtin_amdgcn_raw_buffer_store_b64(u32x2{__float_as_uint(g4 ? acc[1] : acc[0]), tag}, rws, o0, 0, AUX_SC1);
      }
    }
  } else if (w == 8) {
    lds_wait4(L, FL_WQ);
    float s0 = 0.f, s1 = 0.f;
#pragma unroll
    for (int r = 0; r < 2; ++r) {
      const u32 xo = L_X + (r ? 64 + (lane & 7) : lane) * 16;
      const u32x4 x0 = *(LAS u32x4*)(L + xo), x1 = *(LAS u32x4*)(L + xo + 1152);
      float t0 = 0.f, t1 = 0.f;
#pragma unroll
      for (int j = 0; j < 4; ++j) {
        const float glo = bf_lo(ggw[r][j]), ghi = bf_hi(ggw[r][j]);
        t0 += rbf(bf_lo(x0[j]) * glo) + rbf(bf_hi(x0[j]) * ghi);
        t1 += rbf(bf_lo(x1[j]) * glo) + rbf(bf_hi(x1[j]) * ghi);
      }
      if (r == 0 || lane < 8) { s0 += t0; s1 += t1; }
    }
    s0 += dpp<XOR1>(s0); s0 += dpp<XOR2>(s0); s0 += dpp<ROR4>(s0); s0 += dpp<ROR8>(s0); s0 = x32_sum(x16_sum(s0));
    s1 += dpp<XOR1>(s1); s1 += dpp<XOR2>(s1); s1 += dpp<ROR4>(s1); s1 += dpp<ROR8>(s1); s1 = x32_sum(x16_sum(s1));
    const float g0 = rbf(1.f / (1.f + __expf(-rbf(s0)))), g1 = rbf(1.f / (1.f + __expf(-rbf(s1))));
    if (lane == 0) *(LAS f32x4*)(L + L_G) = f32x4{g0, g1, rbf(1.f - g0), rbf(1.f - g1)};
  }

  lds_wait4(L, FL_Q);
  const u32x4 q0 = *(LAS u32x4*)(L + L_QS + col * 16), q1 = *(LAS u32x4*)(L + L_QS + 256 + col * 16);
  float x[16];
#pragma unroll
  for (int i = 0; i < 8; ++i) {
    float d0 = 0.f, d1 = 0.f;
#pragma unroll
    for (int j = 0; j < 4; ++j) { d0 = dot2(kk[i][j], q0[j], d0); d1 = dot2(kk[i][j], q1[j], d1); }
    x[2 * i] = d0; x[2 * i + 1] = d1;
  }
#define TR_STEP(HALF, CTRL, BIT) _Pragma("unroll") for (int h = 0; h < (HALF); ++h) { \
    const float lo_ = x[h], hi_ = x[h + (HALF)]; const float t1_ = lo_ + dpp<CTRL>(lo_), t2_ = hi_ + dpp<CTRL>(hi_); \
    x[h] = (col & (BIT)) ? t2_ : t1_; }
  TR_STEP(8, ROR8, 8) TR_STEP(4, XOR7, 4) TR_STEP(2, XOR2, 2) TR_STEP(1, XOR1, 1)
#undef TR_STEP
  const float sc = rbf(rbf(x[0]) * 0.08838834764831845f) * LOG2E;
  float mx = sc;
  mx = fmaxf(mx, dpp<XOR2>(mx)); mx = fmaxf(mx, dpp<ROR4>(mx)); mx = fmaxf(mx, dpp<ROR8>(mx)); mx = x32_max(x16_max(mx));
  const float pr = __builtin_amdgcn_exp2f(sc - mx);
  float ls = pr;
  ls += dpp<XOR2>(ls); ls += dpp<ROR4>(ls); ls += dpp<ROR8>(ls); ls = x32_sum(x16_sum(ls));
  if (lane < 2) { *(LAS float*)(L + L_MM + lane * 64 + w * 4) = mx; *(LAS float*)(L + L_ML + lane * 64 + w * 4) = ls; }
  f32x2 a0[4], a1[4];
#pragma unroll
  for (int j = 0; j < 4; ++j) { a0[j] = f32x2{0.f, 0.f}; a1[j] = f32x2{0.f, 0.f}; }
#define PV(i) { const float p0_ = dpp<BCAST + 2 * (i)>(pr), p1_ = dpp<BCAST + 2 * (i) + 1>(pr); \
    _Pragma("unroll") for (int j = 0; j < 4; ++j) { const f32x2 vf = {bf_lo(vv[i][j]), bf_hi(vv[i][j])}; \
      a0[j] = __builtin_elementwise_fma(vf, f32x2{p0_, p0_}, a0[j]); a1[j] = __builtin_elementwise_fma(vf, f32x2{p1_, p1_}, a1[j]); } }
  PV(0) PV(1) PV(2) PV(3) PV(4) PV(5) PV(6) PV(7)
#undef PV
  float y[8];
#pragma unroll
  for (int k = 0; k < 8; ++k) {
    auto r = __builtin_amdgcn_permlane32_swap(__float_as_uint(a0[k >> 1][k & 1]), __float_as_uint(a1[k >> 1][k & 1]), false, false);
    y[k] = __uint_as_float(r[0]) + __uint_as_float(r[1]);
  }
  f32x4 z;
#pragma unroll
  for (int k = 0; k < 4; ++k) {
    auto r = __builtin_amdgcn_permlane16_swap(__float_as_uint(y[k]), __float_as_uint(y[k + 4]), false, false);
    z[k] = __uint_as_float(r[0]) + __uint_as_float(r[1]);
  }
  *(LAS f32x4*)(L + L_MRG + w * 1024 + ((g4 >> 1) * 128 + 8 * col + 4 * (g4 & 1)) * 4) = z;
  asm volatile("s_waitcnt lgkmcnt(0)" ::: "memory");
  if (lane == 0) lds_inc(L, C_DONE);

  if (w == 5) {
    const int cc = lane < 36 ? lane : 35;
    const u32 ho = WS_HP + (36 * u + cc) * 8;
    u32x2 hg[6];
    const float bb = bf_lo(p.b1[36 * u + cc]);
#pragma unroll 1
    for (u32 spin = 0;; ++spin) {
      asm volatile("" ::: "memory");
#pragma unroll
      for (int j = 0; j < 6; ++j) hg[j] = __builtin_amdgcn_raw_buffer_load_b64(rws, ho + j * F * 8, 0, AUX_SC1);
      bool ok = true;
#pragma unroll
      for (int j = 0; j < 6; ++j) ok &= hg[j][1] == tag;
      if (__all(ok) || spin > SPIN_CAP) break;
      __builtin_amdgcn_s_sleep(16);
    }
#pragma unroll
    for (int bt = 0; bt < 2; ++bt) {
      float hs = __uint_as_float(hg[bt][0]) + __uint_as_float(hg[2 + bt][0]) + __uint_as_float(hg[4 + bt][0]);
      hs = rbf(rbf(hs) + bb);
      hs = hs > 0.f ? hs : 0.f;
      const u32 hv = lane < 36 ? (cvtpk(hs * hs, 0.f) & 0xffffu) : 0u;
      if (lane < 48) *(LAS u16*)(L + L_AH + bt * 96 + lane * 2) = (u16)hv;
    }
  }
  u32 bias2 = 0;
  if (w == 0 && wg < 144 && lane < 4) bias2 = *(const GAS u32*)(p.b2 + fcg * 288 + ch * 8 + 2 * lane);

  if (w >= 5 && w < 9) {
    lds_wait16<true>(L, C_ISSUED);
    const char* bo = (const char*)p.wo + (size_t)(hc * 256 + 32 * rb) * 1152 + cg * 576;
#pragma unroll 1
    for (int j = w - 5; j < 18; j += 4) { const u32 c = 64 * j + lane, row = (c * 1821u) >> 16; glds16<true>(bo, row * 576 + c * 16, lb + L_WO + j * 1024); }
    const char* b2 = (const char*)p.w2 + (size_t)(36 * u) * 1152 + cg * 576;
#pragma unroll 1
    for (int j = w - 5; j < 21; j += 4) { const u32 c = 64 * j + lane, row = (c * 1821u) >> 16; if (c < 1296) glds16<true>(b2, row * 576 + c * 16, lb + L_W2 + j * 1024); }
    asm volatile("s_waitcnt vmcnt(0) lgkmcnt(0)" ::: "memory");
    if (lane == 0) lds_set(L, FL_WD + (w - 5) * 4, 1u);
  }

  if (w < 4) {
    const int h = w >> 1, d = tid & 127;
    lds_wait16<false>(L, C_DONE);
    if (lane < 4) __builtin_amdgcn_raw_buffer_store_b32(tag, rws, WS_F1 + ((hkv * 16 + w * 4 + lane) * 32 + ii) * 4, 0, AUX_SC1);
    float ov[16];
#pragma unroll
    for (int j = 0; j < 16; ++j) ov[j] = *(LAS float*)(L + L_MRG + j * 1024 + tid * 4);
    const float mj = *(LAS float*)(L + L_MM + h * 64 + col * 4), lj = *(LAS float*)(L + L_ML + h * 64 + col * 4);
    float M = mj;
    M = fmaxf(M, dpp<XOR1>(M)); M = fmaxf(M, dpp<XOR2>(M)); M = fmaxf(M, dpp<ROR4>(M)); M = fmaxf(M, dpp<ROR8>(M));
    const float e = __builtin_amdgcn_exp2f(mj - M);
    float Ls = e * lj;
    Ls += dpp<XOR1>(Ls); Ls += dpp<XOR2>(Ls); Ls += dpp<ROR4>(Ls); Ls += dpp<ROR8>(Ls);
    float o = 0.f;
#pragma unroll
    for (int j = 0; j < 16; ++j) o = fmaf(__uint_as_float(__builtin_amdgcn_readlane(__float_as_uint(e), j)), ov[j], o);
    const u32 rec = WS_P1 + (((hkv * 8 + h * 4 + (d >> 5)) * 16) + b * 8 + s) * 288;
    __builtin_amdgcn_raw_buffer_store_b64(u32x2{__float_as_uint(o), tag}, rws, rec + (d & 31) * 8, 0, AUX_SC1);
    if ((d & 31) < 2) __builtin_amdgcn_raw_buffer_store_b64(u32x2{__float_as_uint((d & 1) ? Ls : M), tag}, rws, rec + 256 + (d & 1) * 8, 0, AUX_SC1);
  } else if (w == 4) {
    const int gb = lane >> 5, gs = (lane >> 2) & 7, dq = lane & 3;
    const u32 rec = WS_P1 + (((hc * 8 + rb) * 16) + gb * 8 + gs) * 288;
    lds_wait16<true>(L, C_DONE);
    wait_hint(rws, WS_F1 + ((hc * 16 + ii) * 32 + (lane & 15)) * 4, tag);
    if (rb == 0 && lane < NREP) __builtin_amdgcn_raw_buffer_store_b32(tag, rws, WS_F2 + ((cg * NREP + lane) * 32 + hc) * 4, 0, AUX_SC1);
    u32x4 og[4], ml;
#pragma unroll 1
    for (u32 spin = 0;; ++spin) {
      asm volatile("" ::: "memory");
#pragma unroll
      for (int j = 0; j < 4; ++j) og[j] = __builtin_amdgcn_raw_buffer_load_b128(rws, rec + dq * 64 + j * 16, 0, AUX_SC1);
      ml = __builtin_amdgcn_raw_buffer_load_b128(rws, rec + 256, 0, AUX_SC1);
      bool ok = (ml[1] == tag) & (ml[3] == tag);
#pragma unroll
      for (int j = 0; j < 4; ++j) ok &= (og[j][1] == tag) & (og[j][3] == tag);
      if (__all(ok) || spin > SPIN_CAP) break;
      __builtin_amdgcn_s_sleep(4);
    }
    const float m = __uint_as_float(ml[0]);
    float M = m;
    M = fmaxf(M, dpp<ROR4>(M)); M = fmaxf(M, dpp<ROR8>(M)); M = x16_max(M);
    const float e = __builtin_amdgcn_exp2f(m - M);
    float Ls = e * __uint_as_float(ml[2]);
    Ls += dpp<ROR4>(Ls); Ls += dpp<ROR8>(Ls); Ls = x16_sum(Ls);
    const float inv = 1.f / Ls;
    float ov[8];
#pragma unroll
    for (int k = 0; k < 8; ++k) {
      float t = e * __uint_as_float(og[k >> 1][(k & 1) * 2]);
      t += dpp<ROR4>(t); t += dpp<ROR8>(t); t = x16_sum(t);
      ov[k] = t * inv;
    }
    if (gs == 0) *(LAS u32x4*)(L + L_AO + gb * 64 + dq * 16) = u32x4{cvtpk(ov[0], ov[1]), cvtpk(ov[2], ov[3]), cvtpk(ov[4], ov[5]), cvtpk(ov[6], ov[7])};
  }

  if (w < 10 && w != 4) {
    const int mw = w < 4 ? w : w - 1;
    const u32 ao = L_AO + (lane & 1) * 64 + 4 * g4 * 2, ah = L_AH + (lane & 1) * 96 + 4 * g4 * 2;
    const int rr = 4 * g4 + q4, tb = g4 & 1, tt = g4 >> 1;
    lds_wait4(L, FL_WD);
    f32x4 df[2], da[2];
    s16x4 bo[2][2];
#pragma unroll
    for (int t = 0; t < 2; ++t) { df[t] = f32x4{0.f, 0.f, 0.f, 0.f}; da[t] = f32x4{0.f, 0.f, 0.f, 0.f}; }
#pragma unroll
    for (int st = 0; st < 3; ++st) {
      const s16x4 a = lds_b64(L, ah + st * 32);
      const int row = min(16 * st + rr, 35);
#pragma unroll
      for (int t = 0; t < 2; ++t) df[t] = mfma16(a, lds_tr(L, L_W2 + row * 576 + (2 * mw + t) * 32 + p4 * 8), df[t]);
    }
#pragma unroll
    for (int st = 0; st < 2; ++st)
#pragma unroll
      for (int t = 0; t < 2; ++t) bo[st][t] = lds_tr(L, L_WO + (16 * st + rr) * 576 + (2 * mw + t) * 32 + p4 * 8);
    const f32x4 gt = *(const LAS f32x4*)(L + L_G);
    const float ga = tb ? gt[1] : gt[0];
    const float vf = (tb ? gt[3] : gt[2]) * (tt ? (tb ? df[1][1] : df[1][0]) : (tb ? df[0][1] : df[0][0]));
    const int n = (2 * mw + tt) * 16 + col;
    const u32 off = WS_P2 + ((((cg * 2 + tb) * 36 + (n >> 3)) * 128) + u) * 64 + (n & 7) * 8;
    asm volatile("" :: "v"(vf), "v"(off));
    asm volatile("s_waitcnt lgkmcnt(0)\n\ts_barrier" ::: "memory");
#pragma unroll
    for (int st = 0; st < 2; ++st) {
      const s16x4 a = lds_b64(L, ao + st * 32);
#pragma unroll
      for (int t = 0; t < 2; ++t) da[t] = mfma16(a, bo[st][t], da[t]);
    }
    const float val = ga * (tt ? (tb ? da[1][1] : da[1][0]) : (tb ? da[0][1] : da[0][0])) + vf;
    __builtin_amdgcn_raw_buffer_store_b64(u32x2{__float_as_uint(val), tag}, rws, off, 0, AUX_SC1);
  } else {
    asm volatile("s_waitcnt lgkmcnt(0)\n\ts_barrier" ::: "memory");
  }

  if (wg >= 144 || w != 0) return;
  {
    const u32 off = WS_P2 + (((fcg * 2 + fb) * 36 + ch) * 128) * 64 + lane * 16;
    wait_hint(rws, WS_F2 + ((fcg * NREP + wg % NREP) * 32 + (lane & 15)) * 4, tag);
    u32x4 pg[8];
#pragma unroll 1
    for (u32 spin = 0;; ++spin) {
      asm volatile("" ::: "memory");
#pragma unroll
      for (int j = 0; j < 8; ++j) pg[j] = __builtin_amdgcn_raw_buffer_load_b128(rws, off + j * 1024, 0, AUX_SC1);
      bool ok = true;
#pragma unroll
      for (int j = 0; j < 8; ++j) ok &= (pg[j][1] == tag) & (pg[j][3] == tag);
      if (__all(ok) || spin > SPIN_CAP) break;
      __builtin_amdgcn_s_sleep(4);
    }
    float t0 = 0.f, t1 = 0.f;
#pragma unroll
    for (int j = 0; j < 8; ++j) { t0 += __uint_as_float(pg[j][0]); t1 += __uint_as_float(pg[j][2]); }
    t0 += dpp<ROR4>(t0); t0 += dpp<ROR8>(t0); t0 = x32_sum(x16_sum(t0));
    t1 += dpp<ROR4>(t1); t1 += dpp<ROR8>(t1); t1 = x32_sum(x16_sum(t1));
    const float omg = ((const LAS float*)(L + L_G))[2 + fb];
    t0 += omg * bf_lo(bias2); t1 += omg * bf_hi(bias2);
    if (lane < 4) *(GAS u32*)(p.out + fb * D + fcg * 288 + ch * 8 + 2 * lane) = cvtpk(t0, t1);
    if (wg == 0 && lane == 0) __hip_atomic_store((GAS u32*)(p.ws + WS_E), tag, __ATOMIC_RELAXED, __HIP_MEMORY_SCOPE_AGENT);
  }
}

extern "C" void kernel_launch(void* const* d_in, const int* in_sizes, int n_in, void* d_out, int out_size, void* d_ws, size_t ws_size,
                              hipStream_t stream) {
  Params p;
  p.x = (const u16*)d_in[0]; p.K = (const u16*)d_in[1]; p.V = (const u16*)d_in[2]; p.wq = (const u16*)d_in[3]; p.wo = (const u16*)d_in[4];
  p.w1 = (const u16*)d_in[6]; p.b1 = (const u16*)d_in[7]; p.w2 = (const u16*)d_in[8]; p.b2 = (const u16*)d_in[9]; p.gw = (const u16*)d_in[10];
  p.out = (u16*)d_out; p.ws = (unsigned char*)d_ws;
  (void)hipFuncSetAttribute((const void*)fused_kernel, hipFuncAttributeMaxDynamicSharedMemorySize, L_END);
  hipLaunchKernelGGL(fused_kernel, dim3(NWG), dim3(NTHR), L_END, stream, p);
}
```

```cpp
#include <hip/hip_runtime.h>
#include <stdint.h>

typedef unsigned short u16;
typedef unsigned int u32;
typedef unsigned long long u64;
typedef __attribute__((ext_vector_type(4))) unsigned u32x4;
typedef __attribute__((ext_vector_type(2))) unsigned u32x2;
typedef __attribute__((ext_vector_type(4))) float f32x4;
typedef __attribute__((ext_vector_type(2))) float f32x2;
typedef __attribute__((ext_vector_type(4))) short s16x4;
typedef __attribute__((ext_vector_type(2))) __bf16 bf16x2;
#define GAS __attribute__((address_space(1)))
#define LAS __attribute__((address_space(3)))

constexpr int D = 576, HKV = 16, SK = 32768, DH = 128, WIN = 4096, F = 4608;
constexpr int NWG = 256, NTHR = 1024;
constexpr int NREP = 18;
constexpr u32 QDELAY = 220;
constexpr u32 SPIN_CAP = 1u << 16;
constexpr u32 LDS_CAP = 1u << 22;

constexpr u32 WS_E = 0;
constexpr u32 WS_QP = 256;
constexpr u32 WS_HP = WS_QP + 16 * 2 * 4 * 256 * 8;
constexpr u32 WS_P1 = WS_HP + 3 * 2 * F * 8;
constexpr u32 WS_P2 = WS_P1 + 16 * 8 * 16 * 288;
constexpr u32 WS_F1 = WS_P2 + 2 * 2 * 36 * 128 * 64;
constexpr u32 WS_F2 = WS_F1 + 16 * 16 * 128;
constexpr u32 WS_END = WS_F2 + 2 * NREP * 128;

constexpr u32 L_X = 0, L_WQ = 3072, L_W1 = L_WQ + 18432, L_WO = L_W1 + 24576, L_W2 = L_WO + 18432;
constexpr u32 L_MRG = L_W2 + 21504;
constexpr u32 L_MM = L_MRG + 16384;
constexpr u32 L_ML = L_MM + 128;
constexpr u32 L_QS = L_ML + 128;
constexpr u32 L_AO = L_QS + 512;
constexpr u32 L_AH = L_AO + 128;
constexpr u32 L_G = L_AH + 192;
constexpr u32 L_FLAG = L_G + 16;
constexpr u32 C_ISSUED = L_FLAG, C_DONE = L_FLAG + 4;
constexpr u32 FL_WQ = L_FLAG + 16, FL_W1 = L_FLAG + 32, FL_Q = L_FLAG + 48, FL_WD = L_FLAG + 64;
constexpr u32 L_JUNK = L_FLAG + 128;
constexpr u32 L_END = L_JUNK + 256;

struct Params {
  const u16* x; const u16* K; const u16* V; const u16* wq; const u16* wo; const u16* w1;
  const u16* b1; const u16* w2; const u16* b2; const u16* gw; u16* out; unsigned char* ws;
};

constexpr int AUX_NT = 2, AUX_SC1 = 16;
constexpr float LOG2E = 1.4426950408889634f;

__device__ __forceinline__ float bf_lo(u32 v) { return __uint_as_float(v << 16); }
__device__ __forceinline__ float bf_hi(u32 v) { return __uint_as_float(v & 0xffff0000u); }
__device__ __forceinline__ u32 cvtpk(float lo, float hi) {
  u32 r; asm("v_cvt_pk_bf16_f32 %0, %1, %2" : "=v"(r) : "v"(lo), "v"(hi)); return r;
}
__device__ __forceinline__ float rbf(float f) { return __uint_as_float(cvtpk(f, f) << 16); }
__device__ __forceinline__ float dot2(u32 a, u32 b, float acc) {
  return __builtin_amdgcn_fdot2_f32_bf16(__builtin_bit_cast(bf16x2, a), __builtin_bit_cast(bf16x2, b), acc, false);
}
template <int CTRL> __device__ __forceinline__ float dpp(float x) {
  return __builtin_bit_cast(float, __builtin_amdgcn_mov_dpp(__builtin_bit_cast(int, x), CTRL, 0xf, 0xf, true));
}
constexpr int XOR1 = 0xB1, XOR2 = 0x4E, XOR7 = 0x141, ROR4 = 0x124, ROR8 = 0x128, BCAST = 0x150;
__device__ __forceinline__ float x16_max(float x) {
  auto s = __builtin_amdgcn_permlane16_swap(__float_as_uint(x), __float_as_uint(x), false, false);
  return fmaxf(__uint_as_float(s[0]), __uint_as_float(s[1]));
}
__device__ __forceinline__ float x32_max(float x) {
  auto s = __builtin_amdgcn_permlane32_swap(__float_as_uint(x), __float_as_uint(x), false, false);
  return fmaxf(__uint_as_float(s[0]), __uint_as_float(s[1]));
}
__device__ __forceinline__ float x16_sum(float x) {
  auto s = __builtin_amdgcn_permlane16_swap(__float_as_uint(x), __float_as_uint(x), false, false);
  return __uint_as_float(s[0]) + __uint_as_float(s[1]);
}
__device__ __forceinline__ float x32_sum(float x) {
  auto s = __builtin_amdgcn_permlane32_swap(__float_as_uint(x), __float_as_uint(x), false, false);
  return __uint_as_float(s[0]) + __uint_as_float(s[1]);
}
template <bool NT> __device__ __forceinline__ void glds16(const void* sbase, u32 voff, u32 lds_dst) {
  unsigned keep;
  if (NT)
    asm volatile("s_mov_b32 %0, m0\n\ts_mov_b32 m0, %3\n\ts_nop 0\n\tglobal_load_lds_dwordx4 %1, %2 nt\n\ts_mov_b32 m0, %0" : "=&s"(keep) : "v"(voff), "s"(sbase), "s"(lds_dst) : "memory");
  else
    asm volatile("s_mov_b32 %0, m0\n\ts_mov_b32 m0, %3\n\ts_nop 0\n\tglobal_load_lds_dwordx4 %1, %2\n\ts_mov_b32 m0, %0" : "=&s"(keep) : "v"(voff), "s"(sbase), "s"(lds_dst) : "memory");
}
__device__ __forceinline__ f32x4 mfma16(s16x4 a, s16x4 b, f32x4 c) { return __builtin_amdgcn_mfma_f32_16x16x16bf16_1k(a, b, c, 0, 0, 0); }
__device__ __forceinline__ s16x4 lds_tr(LAS unsigned char* L, u32 off) { return __builtin_amdgcn_ds_read_tr16_b64_v4i16((LAS s16x4*)(L + off)); }
__device__ __forceinline__ s16x4 lds_b64(LAS unsigned char* L, u32 off) { return *(LAS s16x4*)(L + off); }
__device__ __forceinline__ u32 lds_get(LAS unsigned char* L, u32 off) { return __hip_atomic_load((LAS u32*)(L + off), __ATOMIC_RELAXED, __HIP_MEMORY_SCOPE_WORKGROUP); }
__device__ __forceinline__ void lds_set(LAS unsigned char* L, u32 off, u32 v) { __hip_atomic_store((LAS u32*)(L + off), v, __ATOMIC_RELAXED, __HIP_MEMORY_SCOPE_WORKGROUP); }
__device__ __forceinline__ void lds_inc(LAS unsigned char* L, u32 off) { __hip_atomic_fetch_add((LAS u32*)(L + off), 1u, __ATOMIC_RELAXED, __HIP_MEMORY_SCOPE_WORKGROUP); }
__device__ __forceinline__ void lds_wait4(LAS unsigned char* L, u32 off) {
#pragma unroll 1
  for (u32 spin = 0; spin < LDS_CAP; ++spin) {
    if (__all(lds_get(L, off + (__lane_id() & 3) * 4) != 0u)) break;
    __builtin_amdgcn_s_sleep(1);
  }
  asm volatile("" ::: "memory");
}
template <bool SLEEP> __device__ __forceinline__ void lds_wait16(LAS unsigned char* L, u32 off) {
#pragma unroll 1
  for (u32 spin = 0; spin < LDS_CAP; ++spin) {
    if (lds_get(L, off) == 16u) break;
    if (SLEEP) __builtin_amdgcn_s_sleep(1);
  }
  asm volatile("" ::: "memory");
}
__device__ __forceinline__ void wait_hint(__amdgpu_buffer_rsrc_t r, u32 off, u32 tag) {
#pragma unroll 1
  for (u32 spin = 0; spin < SPIN_CAP; ++spin) {
    asm volatile("" ::: "memory");
    if (__all(__builtin_amdgcn_raw_buffer_load_b32(r, off, 0, AUX_SC1) == tag)) break;
  }
}

__global__ void __launch_bounds__(NTHR, 4) fused_kernel(Params p) {
  extern __shared__ __attribute__((aligned(16))) unsigned char smem[];
  LAS unsigned char* L = (LAS unsigned char*)smem;
  const u32 lb = (u32)(uintptr_t)L;
  const int tid = threadIdx.x, lane = tid & 63, w = __builtin_amdgcn_readfirstlane(tid >> 6);
  const int wg = blockIdx.x;
  const int idx = wg >> 3, hkv = (wg & 7) * 2 + (idx >> 4), ii = idx & 15, b = ii >> 3, s = ii & 7;
  const int hc = (hkv + 2) & 15, rb = ii >> 1, cg = ii & 1, u = hc * 8 + rb;
  const int fb = wg / 72, c72 = wg - 72 * fb, fcg = c72 / 36, ch = c72 - 36 * fcg;
  const __amdgpu_buffer_rsrc_t rws = __builtin_amdgcn_make_buffer_rsrc((void*)p.ws, 0, WS_END, 0x00020000);
  const int g4 = lane >> 4, col = lane & 15, q4 = col >> 2, p4 = lane & 3;

  if (w < 4) {
    const int cb = ii & 3, kq = ii >> 2;
    const u32 voff = (lane >> 3) * 8192 + (lane & 7) * 16;
    const char* base = (const char*)p.wq + (size_t)(144 * kq + 8 * w) * 8192 + (hkv * 256 + 64 * cb) * 2;
    if (w < 2) glds16<false>(p.x, w * 1024 + lane * 16, lb + L_X + w * 1024);
    else if (w == 2) { if (lane < 16) glds16<false>(p.x, 2048 + lane * 16, lb + L_X + 2048); }
#pragma unroll 1
    for (int j = w; j < 18; j += 4) { glds16<false>(base, voff, lb + L_WQ + j * 1024); base += 32 * 8192; }
  } else if (w == 15) {
    if (lane < 32) lds_set(L, L_FLAG + lane * 4, 0u);
    asm volatile("s_waitcnt lgkmcnt(0)" ::: "memory");
  }
  __builtin_amdgcn_s_barrier();
  u32 epoch;
  asm volatile("s_load_dword %0, %1, 0x0" : "=s"(epoch) : "s"(p.ws) : "memory");

  if (w >= 4 && w < 8 && wg < 216) {
    const int cb = wg / 3, kt = wg - 3 * cb;
    const u32 voff = (lane >> 3) * 9216 + (lane & 7) * 16;
    const char* base = (const char*)p.w1 + (size_t)(192 * kt + 8 * (w - 4)) * 9216 + cb * 128;
#pragma unroll 1
    for (int j = w - 4; j < 24; j += 4) { glds16<false>(base, voff, lb + L_W1 + j * 1024); base += 32 * 9216; }
  }
  if (w == 14) {
    u32 o = WS_HP + 36 * u * 8;
    if (lane < 36) o = WS_P2 + (((cg * 2 + (lane >= 18)) * 36 + 2 * (lane >= 18 ? lane - 18 : lane)) * 128 + u) * 64;
    else if (lane < 44) o = WS_P2 + (((fcg * 2 + (fb & 1)) * 36 + ch) * 128) * 64 + (lane - 36) * 1024;
    else if (lane == 44) o = WS_F1 + (hc * 16 + ii) * 128;
    else if (lane == 45) o = WS_F2 + (fcg * NREP + wg % NREP) * 128;
    else if (lane == 46) o = WS_P1 + ((hc * 8 + rb) * 16) * 288;
    else if (lane == 47) o = WS_P1 + ((hkv * 8) * 16 + b * 8 + s) * 288;
    else if (lane == 48) o = WS_P1 + ((hkv * 8 + 7) * 16 + b * 8 + s) * 288;
    else if (lane == 49) o = WS_F1 + hkv * 16 * 128;
    else if (lane == 50) o = WS_F2 + cg * NREP * 128;
    const unsigned char* a = p.ws + o;
    if (lane >= 52 && lane < 56) a = (const unsigned char*)p.wo + (size_t)(hc * 256 + 32 * rb + (lane - 52) * 10) * 1152 + cg * 576;
    else if (lane >= 56 && lane < 60) a = (const unsigned char*)p.w2 + (size_t)(36 * u + (lane - 56) * 11) * 1152 + cg * 576;
    else if (lane == 60) a = (const unsigned char*)(p.b1 + 36 * u);
    else if (lane == 61) a = (const unsigned char*)(p.b2 + fcg * 288 + ch * 8);
    else if (lane == 62) a = (const unsigned char*)(p.out + (fb & 1) * D + fcg * 288 + ch * 8);
    unsigned keep;
    asm volatile("s_mov_b32 %0, m0\n\ts_mov_b32 m0, %2\n\ts_nop 0\n\tglobal_load_lds_dword %1, off sc1\n\ts_mov_b32 m0, %0" : "=&s"(keep) : "v"(a), "s"(lb + L_JUNK) : "memory");
  }
  u32x4 ggw[2];
  if (w == 8) {
    const GAS u32x4* gp = (const GAS u32x4*)p.gw;
    ggw[0] = gp[lane]; ggw[1] = gp[64 + (lane & 7)];
  }
  const __amdgpu_buffer_rsrc_t rk = __builtin_amdgcn_make_buffer_rsrc((void*)p.K, 0, 0x10000000, 0x00020000);
  const __amdgpu_buffer_rsrc_t rv = __builtin_amdgcn_make_buffer_rsrc((void*)p.V, 0, 0x10000000, 0x00020000);
  const u32 kvoff = (u32)(((u32)(b * HKV + hkv) * SK + (SK - WIN) + s * 512 + w * 32) * (DH * 2));

  u64 tpub = 0;
  if (w < 4) {
    asm volatile("s_waitcnt vmcnt(0) lgkmcnt(0)" : "+s"(epoch) :: "memory");
    if (lane == 0) lds_set(L, FL_WQ + w * 4, 1u);
    lds_wait4(L, FL_WQ);
    const int cb = ii & 3, kq = ii >> 2;
    const u32 aoff = L_X + (lane & 1) * 1152 + (144 * kq + 4 * g4) * 2;
    const u32 boff = L_WQ + (4 * g4 + q4) * 128 + p4 * 8 + w * 32;
    s16x4 fa[9], fb_[9];
#pragma unroll
    for (int st = 0; st < 9; ++st) { fa[st] = lds_b64(L, aoff + st * 32); fb_[st] = lds_tr(L, boff + st * 2048); }
    f32x4 acc = {0.f, 0.f, 0.f, 0.f};
#pragma unroll
    for (int st = 0; st < 9; ++st) acc = mfma16(fa[st], fb_[st], acc);
    if (lane < 32) {
      const u32 o0 = WS_QP + (((hkv * 2 + g4) * 4 + kq) * 256 + 64 * cb + 16 * w + col) * 8;
      __builtin_amdgcn_raw_buffer_store_b64(u32x2{__float_as_uint(g4 ? acc[1] : acc[0]), epoch + 1u}, rws, o0, 0, AUX_SC1);
    }
    tpub = __builtin_amdgcn_s_memrealtime();
  }

  u32x4 kk[8], vv[8];
#pragma unroll
  for (int j = 0; j < 8; ++j) kk[j] = __builtin_amdgcn_raw_buffer_load_b128(rk, lane * 16, kvoff + j * 1024, AUX_NT);
#pragma unroll
  for (int j = 0; j < 8; ++j) vv[j] = __builtin_amdgcn_raw_buffer_load_b128(rv, lane * 16, kvoff + j * 1024, 0);
  __builtin_amdgcn_sched_barrier(0);
  if (lane == 0) lds_inc(L, C_ISSUED);
  asm volatile("s_waitcnt lgkmcnt(0)" : "+s"(epoch) :: "memory");
  const u32 tag = epoch + 1u;

  if (w < 4) {
    const u32 go = WS_QP + (((hkv * 2 + b) * 4 + (lane >> 5)) * 256 + 64 * w + 2 * (lane & 31)) * 8;
    while (__builtin_amdgcn_s_memrealtime() - tpub < QDELAY) __builtin_amdgcn_s_sleep(1);
    u32x4 qa, qb;
#pragma unroll 1
    for (u32 spin = 0;; ++spin) {
      asm volatile("" ::: "memory");
      qa = __builtin_amdgcn_raw_buffer_load_b128(rws, go, 0, AUX_SC1);
      qb = __builtin_amdgcn_raw_buffer_load_b128(rws, go + 2 * 2048, 0, AUX_SC1);
      if (__all((qa[1] == tag) & (qa[3] == tag) & (qb[1] == tag) & (qb[3] == tag)) || spin > SPIN_CAP) break;
      __builtin_amdgcn_s_sleep(8);
    }
    const float c0 = x32_sum(__uint_as_float(qa[0]) + __uint_as_float(qb[0])), c1 = x32_sum(__uint_as_float(qa[2]) + __uint_as_float(qb[2]));
    if (lane < 32) *(LAS u32*)(L + L_QS + w * 128 + lane * 4) = cvtpk(c0, c1);
    asm volatile("s_waitcnt lgkmcnt(0)" ::: "memory");
    if (lane == 0) lds_set(L, FL_Q + w * 4, 1u);
  } else if (w < 8) {
    asm volatile("s_waitcnt vmcnt(16)" ::: "memory");
    if (lane == 0) lds_set(L, FL_W1 + (w - 4) * 4, 1u);
    if (wg < 216) {
      lds_wait4(L, FL_WQ);
      lds_wait4(L, FL_W1);
      const int cb = wg / 3, kt = wg - 3 * cb;
      const u32 aoff = L_X + (lane & 1) * 1152 + (192 * kt + 4 * g4) * 2;
      const u32 boff = L_W1 + (4 * g4 + q4) * 128 + p4 * 8 + (w - 4) * 32;
      f32x4 acc = {0.f, 0.f, 0.f, 0.f};
#pragma unroll
      for (int hf = 0; hf < 2; ++hf) {
        s16x4 fa[6], fb_[6];
#pragma unroll
        for (int st = 0; st < 6; ++st) { fa[st] = lds_b64(L, aoff + (hf * 6 + st) * 32); fb_[st] = lds_tr(L, boff + (hf * 6 + st) * 2048); }
#pragma unroll
        for (int st = 0; st < 6; ++st) acc = mfma16(fa[st], fb_[st], acc);
      }
      if (lane < 32) {
        const u32 o0 = WS_HP + ((kt * 2 + g4) * F + 64 * cb + 16 * (w - 4) + col) * 8;
        __builtin_amdgcn_raw_buffer_store_b64(u32x2{__float_as_uint(g4 ? acc[1] : acc[0]), tag}, rws, o0, 0, AUX_SC1);
      }
    }
  } else if (w == 8) {
    lds_wait4(L, FL_WQ);
    float s0 = 0.f, s1 = 0.f;
#pragma unroll
    for (int r = 0; r < 2; ++r) {
      const u32 xo = L_X + (r ? 64 + (lane & 7) : lane) * 16;
      const u32x4 x0 = *(LAS u32x4*)(L + xo), x1 = *(LAS u32x4*)(L + xo + 1152);
      float t0 = 0.f, t1 = 0.f;
#pragma unroll
      for (int j = 0; j < 4; ++j) {
        const float glo = bf_lo(ggw[r][j]), ghi = bf_hi(ggw[r][j]);
        t0 += rbf(bf_lo(x0[j]) * glo) + rbf(bf_hi(x0[j]) * ghi);
        t1 += rbf(bf_lo(x1[j]) * glo) + rbf(bf_hi(x1[j]) * ghi);
      }
      if (r == 0 || lane < 8) { s0 += t0; s1 += t1; }
    }
    s0 += dpp<XOR1>(s0); s0 += dpp<XOR2>(s0); s0 += dpp<ROR4>(s0); s0 += dpp<ROR8>(s0); s0 = x32_sum(x16_sum(s0));
    s1 += dpp<XOR1>(s1); s1 += dpp<XOR2>(s1); s1 += dpp<ROR4>(s1); s1 += dpp<ROR8>(s1); s1 = x32_sum(x16_sum(s1));
    const float g0 = rbf(1.f / (1.f + __expf(-rbf(s0)))), g1 = rbf(1.f / (1.f + __expf(-rbf(s1))));
    if (lane == 0) *(LAS f32x4*)(L + L_G) = f32x4{g0, g1, rbf(1.f - g0), rbf(1.f - g1)};
  }

  lds_wait4(L, FL_Q);
  const u32x4 q0 = *(LAS u32x4*)(L + L_QS + col * 16), q1 = *(LAS u32x4*)(L + L_QS + 256 + col * 16);
  float x[16];
#pragma unroll
  for (int i = 0; i < 8; ++i) {
    float d0 = 0.f, d1 = 0.f;
#pragma unroll
    for (int j = 0; j < 4; ++j) { d0 = dot2(kk[i][j], q0[j], d0); d1 = dot2(kk[i][j], q1[j], d1); }
    x[2 * i] = d0; x[2 * i + 1] = d1;
  }
#define TR_STEP(HALF, CTRL, BIT) _Pragma("unroll") for (int h = 0; h < (HALF); ++h) { \
    const float lo_ = x[h], hi_ = x[h + (HALF)]; const float t1_ = lo_ + dpp<CTRL>(lo_), t2_ = hi_ + dpp<CTRL>(hi_); \
    x[h] = (col & (BIT)) ? t2_ : t1_; }
  TR_STEP(8, ROR8, 8) TR_STEP(4, XOR7, 4) TR_STEP(2, XOR2, 2) TR_STEP(1, XOR1, 1)
#undef TR_STEP
  const float sc = rbf(rbf(x[0]) * 0.08838834764831845f) * LOG2E;
  float mx = sc;
  mx = fmaxf(mx, dpp<XOR2>(mx)); mx = fmaxf(mx, dpp<ROR4>(mx)); mx = fmaxf(mx, dpp<ROR8>(mx)); mx = x32_max(x16_max(mx));
  const float pr = __builtin_amdgcn_exp2f(sc - mx);
  float ls = pr;
  ls += dpp<XOR2>(ls); ls += dpp<ROR4>(ls); ls += dpp<ROR8>(ls); ls = x32_sum(x16_sum(ls));
  if (lane < 2) { *(LAS float*)(L + L_MM + lane * 64 + w * 4) = mx; *(LAS float*)(L + L_ML + lane * 64 + w * 4) = ls; }
  f32x2 a0[4], a1[4];
#pragma unroll
  for (int j = 0; j < 4; ++j) { a0[j] = f32x2{0.f, 0.f}; a1[j] = f32x2{0.f, 0.f}; }
#define PV(i) { const float p0_ = dpp<BCAST + 2 * (i)>(pr), p1_ = dpp<BCAST + 2 * (i) + 1>(pr); \
    _Pragma("unroll") for (int j = 0; j < 4; ++j) { const f32x2 vf = {bf_lo(vv[i][j]), bf_hi(vv[i][j])}; \
      a0[j] = __builtin_elementwise_fma(vf, f32x2{p0_, p0_}, a0[j]); a1[j] = __builtin_elementwise_fma(vf, f32x2{p1_, p1_}, a1[j]); } }
  PV(0) PV(1) PV(2) PV(3) PV(4) PV(5) PV(6) PV(7)
#undef PV
  float y[8];
#pragma unroll
  for (int k = 0; k < 8; ++k) {
    auto r = __builtin_amdgcn_permlane32_swap(__float_as_uint(a0[k >> 1][k & 1]), __float_as_uint(a1[k >> 1][k & 1]), false, false);
    y[k] = __uint_as_float(r[0]) + __uint_as_float(r[1]);
  }
  f32x4 z;
#pragma unroll
  for (int k = 0; k < 4; ++k) {
    auto r = __builtin_amdgcn_permlane16_swap(__float_as_uint(y[k]), __float_as_uint(y[k + 4]), false, false);
    z[k] = __uint_as_float(r[0]) + __uint_as_float(r[1]);
  }
  *(LAS f32x4*)(L + L_MRG + w * 1024 + ((g4 >> 1) * 128 + 8 * col + 4 * (g4 & 1)) * 4) = z;
  asm volatile("s_waitcnt lgkmcnt(0)" ::: "memory");
  if (lane == 0) lds_inc(L, C_DONE);

  if (w == 5) {
    const int cc = lane < 36 ? lane : 35;
    const u32 ho = WS_HP + (36 * u + cc) * 8;
    u32x2 hg[6];
    const float bb = bf_lo(p.b1[36 * u + cc]);
#pragma unroll 1
    for (u32 spin = 0;; ++spin) {
      asm volatile("" ::: "memory");
#pragma unroll
      for (int j = 0; j < 6; ++j) hg[j] = __builtin_amdgcn_raw_buffer_load_b64(rws, ho + j * F * 8, 0, AUX_SC1);
      bool ok = true;
#pragma unroll
      for (int j = 0; j < 6; ++j) ok &= hg[j][1] == tag;
      if (__all(ok) || spin > SPIN_CAP) break;
      __builtin_amdgcn_s_sleep(16);
    }
#pragma unroll
    for (int bt = 0; bt < 2; ++bt) {
      float hs = __uint_as_float(hg[bt][0]) + __uint_as_float(hg[2 + bt][0]) + __uint_as_float(hg[4 + bt][0]);
      hs = rbf(rbf(hs) + bb);
      hs = hs > 0.f ? hs : 0.f;
      const u32 hv = lane < 36 ? (cvtpk(hs * hs, 0.f) & 0xffffu) : 0u;
      if (lane < 48) *(LAS u16*)(L + L_AH + bt * 96 + lane * 2) = (u16)hv;
    }
  }
  u32 bias2 = 0;
  if (w == 0 && wg < 144 && lane < 4) bias2 = *(const GAS u32*)(p.b2 + fcg * 288 + ch * 8 + 2 * lane);

  if (w >= 5 && w < 9) {
    lds_wait16<true>(L, C_ISSUED);
    const char* bo = (const char*)p.wo + (size_t)(hc * 256 + 32 * rb) * 1152 + cg * 576;
#pragma unroll 1
    for (int j = w - 5; j < 18; j += 4) { const u32 c = 64 * j + lane, row = (c * 1821u) >> 16; glds16<false>(bo, row * 576 + c * 16, lb + L_WO + j * 1024); }
    const char* b2 = (const char*)p.w2 + (size_t)(36 * u) * 1152 + cg * 576;
#pragma unroll 1
    for (int j = w - 5; j < 21; j += 4) { const u32 c = 64 * j + lane, row = (c * 1821u) >> 16; if (c < 1296) glds16<false>(b2, row * 576 + c * 16, lb + L_W2 + j * 1024); }
    asm volatile("s_waitcnt vmcnt(0) lgkmcnt(0)" ::: "memory");
    if (lane == 0) lds_set(L, FL_WD + (w - 5) * 4, 1u);
  }

  if (w < 4) {
    const int h = w >> 1, d = tid & 127;
    lds_wait16<false>(L, C_DONE);
    if (lane < 4) __builtin_amdgcn_raw_buffer_store_b32(tag, rws, WS_F1 + ((hkv * 16 + w * 4 + lane) * 32 + ii) * 4, 0, AUX_SC1);
    float ov[16];
#pragma unroll
    for (int j = 0; j < 16; ++j) ov[j] = *(LAS float*)(L + L_MRG + j * 1024 + tid * 4);
    const float mj = *(LAS float*)(L + L_MM + h * 64 + col * 4), lj = *(LAS float*)(L + L_ML + h * 64 + col * 4);
    float M = mj;
    M = fmaxf(M, dpp<XOR1>(M)); M = fmaxf(M, dpp<XOR2>(M)); M = fmaxf(M, dpp<ROR4>(M)); M = fmaxf(M, dpp<ROR8>(M));
    const float e = __builtin_amdgcn_exp2f(mj - M);
    float Ls = e * lj;
    Ls += dpp<XOR1>(Ls); Ls += dpp<XOR2>(Ls); Ls += dpp<ROR4>(Ls); Ls += dpp<ROR8>(Ls);
    float o = 0.f;
#pragma unroll
    for (int j = 0; j < 16; ++j) o = fmaf(__uint_as_float(__builtin_amdgcn_readlane(__float_as_uint(e), j)), ov[j], o);
    const u32 rec = WS_P1 + (((hkv * 8 + h * 4 + (d >> 5)) * 16) + b * 8 + s) * 288;
    __builtin_amdgcn_raw_buffer_store_b64(u32x2{__float_as_uint(o), tag}, rws, rec + (d & 31) * 8, 0, AUX_SC1);
    if ((d & 31) < 2) __builtin_amdgcn_raw_buffer_store_b64(u32x2{__float_as_uint((d & 1) ? Ls : M), tag}, rws, rec + 256 + (d & 1) * 8, 0, AUX_SC1);
  } else if (w == 4) {
    const int gb = lane >> 5, gs = (lane >> 2) & 7, dq = lane & 3;
    const u32 rec = WS_P1 + (((hc * 8 + rb) * 16) + gb * 8 + gs) * 288;
    lds_wait16<true>(L, C_DONE);
    wait_hint(rws, WS_F1 + ((hc * 16 + ii) * 32 + (lane & 15)) * 4, tag);
    if (rb == 0 && lane < NREP) __builtin_amdgcn_raw_buffer_store_b32(tag, rws, WS_F2 + ((cg * NREP + lane) * 32 + hc) * 4, 0, AUX_SC1);
    u32x4 og[4], ml;
#pragma unroll 1
    for (u32 spin = 0;; ++spin) {
      asm volatile("" ::: "memory");
#pragma unroll
      for (int j = 0; j < 4; ++j) og[j] = __builtin_amdgcn_raw_buffer_load_b128(rws, rec + dq * 64 + j * 16, 0, AUX_SC1);
      ml = __builtin_amdgcn_raw_buffer_load_b128(rws, rec + 256, 0, AUX_SC1);
      bool ok = (ml[1] == tag) & (ml[3] == tag);
#pragma unroll
      for (int j = 0; j < 4; ++j) ok &= (og[j][1] == tag) & (og[j][3] == tag);
      if (__all(ok) || spin > SPIN_CAP) break;
      __builtin_amdgcn_s_sleep(4);
    }
    const float m = __uint_as_float(ml[0]);
    float M = m;
    M = fmaxf(M, dpp<ROR4>(M)); M = fmaxf(M, dpp<ROR8>(M)); M = x16_max(M);
    const float e = __builtin_amdgcn_exp2f(m - M);
    float Ls = e * __uint_as_float(ml[2]);
    Ls += dpp<ROR4>(Ls); Ls += dpp<ROR8>(Ls); Ls = x16_sum(Ls);
    const float inv = 1.f / Ls;
    float ov[8];
#pragma unroll
    for (int k = 0; k < 8; ++k) {
      float t = e * __uint_as_float(og[k >> 1][(k & 1) * 2]);
      t += dpp<ROR4>(t); t += dpp<ROR8>(t); t = x16_sum(t);
      ov[k] = t * inv;
    }
    if (gs == 0) *(LAS u32x4*)(L + L_AO + gb * 64 + dq * 16) = u32x4{cvtpk(ov[0], ov[1]), cvtpk(ov[2], ov[3]), cvtpk(ov[4], ov[5]), cvtpk(ov[6], ov[7])};
  }

  if (w < 10 && w != 4) {
    const int mw = w < 4 ? w : w - 1;
    const u32 ao = L_AO + (lane & 1) * 64 + 4 * g4 * 2, ah = L_AH + (lane & 1) * 96 + 4 * g4 * 2;
    const int rr = 4 * g4 + q4, tb = g4 & 1, tt = g4 >> 1;
    lds_wait4(L, FL_WD);
    f32x4 df[2], da[2];
    s16x4 bo[2][2];
#pragma unroll
    for (int t = 0; t < 2; ++t) { df[t] = f32x4{0.f, 0.f, 0.f, 0.f}; da[t] = f32x4{0.f, 0.f, 0.f, 0.f}; }
#pragma unroll
    for (int st = 0; st < 3; ++st) {
      const s16x4 a = lds_b64(L, ah + st * 32);
      const int row = min(16 * st + rr, 35);
#pragma unroll
      for (int t = 0; t < 2; ++t) df[t] = mfma16(a, lds_tr(L, L_W2 + row * 576 + (2 * mw + t) * 32 + p4 * 8), df[t]);
    }
#pragma unroll
    for (int st = 0; st < 2; ++st)
#pragma unroll
      for (int t = 0; t < 2; ++t) bo[st][t] = lds_tr(L, L_WO + (16 * st + rr) * 576 + (2 * mw + t) * 32 + p4 * 8);
    const f32x4 gt = *(const LAS f32x4*)(L + L_G);
    const float ga = tb ? gt[1] : gt[0];
    const float vf = (tb ? gt[3] : gt[2]) * (tt ? (tb ? df[1][1] : df[1][0]) : (tb ? df[0][1] : df[0][0]));
    const int n = (2 * mw + tt) * 16 + col;
    const u32 off = WS_P2 + ((((cg * 2 + tb) * 36 + (n >> 3)) * 128) + u) * 64 + (n & 7) * 8;
    asm volatile("" :: "v"(vf), "v"(off));
    asm volatile("s_waitcnt lgkmcnt(0)\n\ts_barrier" ::: "memory");
#pragma unroll
    for (int st = 0; st < 2; ++st) {
      const s16x4 a = lds_b64(L, ao + st * 32);
#pragma unroll
      for (int t = 0; t < 2; ++t) da[t] = mfma16(a, bo[st][t], da[t]);
    }
    const float val = ga * (tt ? (tb ? da[1][1] : da[1][0]) : (tb ? da[0][1] : da[0][0])) + vf;
    __builtin_amdgcn_raw_buffer_store_b64(u32x2{__float_as_uint(val), tag}, rws, off, 0, AUX_SC1);
  } else {
    asm volatile("s_waitcnt lgkmcnt(0)\n\ts_barrier" ::: "memory");
  }

  if (wg >= 144 || w != 0) return;
  {
    const u32 off = WS_P2 + (((fcg * 2 + fb) * 36 + ch) * 128) * 64 + lane * 16;
    wait_hint(rws, WS_F2 + ((fcg * NREP + wg % NREP) * 32 + (lane & 15)) * 4, tag);
    u32x4 pg[8];
#pragma unroll 1
    for (u32 spin = 0;; ++spin) {
      asm volatile("" ::: "memory");
#pragma unroll
      for (int j = 0; j < 8; ++j) pg[j] = __builtin_amdgcn_raw_buffer_load_b128(rws, off + j * 1024, 0, AUX_SC1);
      bool ok = true;
#pragma unroll
      for (int j = 0; j < 8; ++j) ok &= (pg[j][1] == tag) & (pg[j][3] == tag);
      if (__all(ok) || spin > SPIN_CAP) break;
      __builtin_amdgcn_s_sleep(4);
    }
    float t0 = 0.f, t1 = 0.f;
#pragma unroll
    for (int j = 0; j < 8; ++j) { t0 += __uint_as_float(pg[j][0]); t1 += __uint_as_float(pg[j][2]); }
    t0 += dpp<ROR4>(t0); t0 += dpp<ROR8>(t0); t0 = x32_sum(x16_sum(t0));
    t1 += dpp<ROR4>(t1); t1 += dpp<ROR8>(t1); t1 = x32_sum(x16_sum(t1));
    const float omg = ((const LAS float*)(L + L_G))[2 + fb];
    t0 += omg * bf_lo(bias2); t1 += omg * bf_hi(bias2);
    if (lane < 4) *(GAS u32*)(p.out + fb * D + fcg * 288 + ch * 8 + 2 * lane) = cvtpk(t0, t1);
    if (wg == 0 && lane == 0) __hip_atomic_store((GAS u32*)(p.ws + WS_E), tag, __ATOMIC_RELAXED, __HIP_MEMORY_SCOPE_AGENT);
  }
}

extern "C" void kernel_launch(void* const* d_in, const int* in_sizes, int n_in, void* d_out, int out_size, void* d_ws, size_t ws_size,
                              hipStream_t stream) {
  Params p;
  p.x = (const u16*)d_in[0]; p.K = (const u16*)d_in[1]; p.V = (const u16*)d_in[2]; p.wq = (const u16*)d_in[3]; p.wo = (const u16*)d_in[4];
  p.w1 = (const u16*)d_in[6]; p.b1 = (const u16*)d_in[7]; p.w2 = (const u16*)d_in[8]; p.b2 = (const u16*)d_in[9]; p.gw = (const u16*)d_in[10];
  p.out = (u16*)d_out; p.ws = (unsigned char*)d_ws;
  (void)hipFuncSetAttribute((const void*)fused_kernel, hipFuncAttributeMaxDynamicSharedMemorySize, L_END);
  hipLaunchKernelGGL(fused_kernel, dim3(NWG), dim3(NTHR), L_END, stream, p);
}
```

```cpp
#include <hip/hip_runtime.h>
#include <stdint.h>

typedef unsigned short u16;
typedef unsigned int u32;
typedef unsigned long long u64;
typedef __attribute__((ext_vector_type(4))) unsigned u32x4;
typedef __attribute__((ext_vector_type(2))) unsigned u32x2;
typedef __attribute__((ext_vector_type(4))) float f32x4;
typedef __attribute__((ext_vector_type(2))) float f32x2;
typedef __attribute__((ext_vector_type(4))) short s16x4;
typedef __attribute__((ext_vector_type(2))) __bf16 bf16x2;
#define GAS __attribute__((address_space(1)))
#define LAS __attribute__((address_space(3)))

constexpr int D = 576, HKV = 16, SK = 32768, DH = 128, WIN = 4096, F = 4608;
constexpr int NWG = 256, NTHR = 1024;
constexpr int NREP = 18;
constexpr u32 QDELAY = 280;
constexpr u32 SPIN_CAP = 1u << 16;
constexpr u32 LDS_CAP = 1u << 22;

constexpr u32 WS_E = 0;
constexpr u32 WS_QP = 256;
constexpr u32 WS_HP = WS_QP + 16 * 2 * 4 * 256 * 8;
constexpr u32 WS_P1 = WS_HP + 3 * 2 * F * 8;
constexpr u32 WS_P2 = WS_P1 + 16 * 8 * 16 * 288;
constexpr u32 WS_F1 = WS_P2 + 2 * 2 * 36 * 128 * 64;
constexpr u32 WS_F2 = WS_F1 + 16 * 16 * 128;
constexpr u32 WS_END = WS_F2 + 2 * NREP * 128;

constexpr u32 L_X = 0, L_WQ = 3072, L_W1 = L_WQ + 18432, L_WO = L_W1 + 24576, L_W2 = L_WO + 18432;
constexpr u32 L_MRG = L_W2 + 21504;
constexpr u32 L_MM = L_MRG + 16384;
constexpr u32 L_ML = L_MM + 128;
constexpr u32 L_QS = L_ML + 128;
constexpr u32 L_AO = L_QS + 512;
constexpr u32 L_AH = L_AO + 128;
constexpr u32 L_G = L_AH + 192;
constexpr u32 L_FLAG = L_G + 16;
constexpr u32 C_ISSUED = L_FLAG, C_DONE = L_FLAG + 4;
constexpr u32 FL_WQ = L_FLAG + 16, FL_W1 = L_FLAG + 32, FL_Q = L_FLAG + 48, FL_WD = L_FLAG + 64;
constexpr u32 L_JUNK = L_FLAG + 128;
constexpr u32 L_END = L_JUNK + 256;

struct Params {
  const u16* x; const u16* K; const u16* V; const u16* wq; const u16* wo; const u16* w1;
  const u16* b1; const u16* w2; const u16* b2; const u16* gw; u16* out; unsigned char* ws;
};

constexpr int AUX_NT = 2, AUX_SC1 = 16;
constexpr float LOG2E = 1.4426950408889634f;

__device__ __forceinline__ float bf_lo(u32 v) { return __uint_as_float(v << 16); }
__device__ __forceinline__ float bf_hi(u32 v) { return __uint_as_float(v & 0xffff0000u); }
__device__ __forceinline__ u32 cvtpk(float lo, float hi) {
  u32 r; asm("v_cvt_pk_bf16_f32 %0, %1, %2" : "=v"(r) : "v"(lo), "v"(hi)); return r;
}
__device__ __forceinline__ float rbf(float f) { return __uint_as_float(cvtpk(f, f) << 16); }
__device__ __forceinline__ float dot2(u32 a, u32 b, float acc) {
  return __builtin_amdgcn_fdot2_f32_bf16(__builtin_bit_cast(bf16x2, a), __builtin_bit_cast(bf16x2, b), acc, false);
}
template <int CTRL> __device__ __forceinline__ float dpp(float x) {
  return __builtin_bit_cast(float, __builtin_amdgcn_mov_dpp(__builtin_bit_cast(int, x), CTRL, 0xf, 0xf, true));
}
constexpr int XOR1 = 0xB1, XOR2 = 0x4E, XOR7 = 0x141, ROR4 = 0x124, ROR8 = 0x128, BCAST = 0x150;
__device__ __forceinline__ float x16_max(float x) {
  auto s = __builtin_amdgcn_permlane16_swap(__float_as_uint(x), __float_as_uint(x), false, false);
  return fmaxf(__uint_as_float(s[0]), __uint_as_float(s[1]));
}
__device__ __forceinline__ float x32_max(float x) {
  auto s = __builtin_amdgcn_permlane32_swap(__float_as_uint(x), __float_as_uint(x), false, false);
  return fmaxf(__uint_as_float(s[0]), __uint_as_float(s[1]));
}
__device__ __forceinline__ float x16_sum(float x) {
  auto s = __builtin_amdgcn_permlane16_swap(__float_as_uint(x), __float_as_uint(x), false, false);
  return __uint_as_float(s[0]) + __uint_as_float(s[1]);
}
__device__ __forceinline__ float x32_sum(float x) {
  auto s = __builtin_amdgcn_permlane32_swap(__float_as_uint(x), __float_as_uint(x), false, false);
  return __uint_as_float(s[0]) + __uint_as_float(s[1]);
}
template <bool NT> __device__ __forceinline__ void glds16(const void* sbase, u32 voff, u32 lds_dst) {
  unsigned keep;
  if (NT)
    asm volatile("s_mov_b32 %0, m0\n\ts_mov_b32 m0, %3\n\ts_nop 0\n\tglobal_load_lds_dwordx4 %1, %2 nt\n\ts_mov_b32 m0, %0" : "=&s"(keep) : "v"(voff), "s"(sbase), "s"(lds_dst) : "memory");
  else
    asm volatile("s_mov_b32 %0, m0\n\ts_mov_b32 m0, %3\n\ts_nop 0\n\tglobal_load_lds_dwordx4 %1, %2\n\ts_mov_b32 m0, %0" : "=&s"(keep) : "v"(voff), "s"(sbase), "s"(lds_dst) : "memory");
}
__device__ __forceinline__ f32x4 mfma16(s16x4 a, s16x4 b, f32x4 c) { return __builtin_amdgcn_mfma_f32_16x16x16bf16_1k(a, b, c, 0, 0, 0); }
__device__ __forceinline__ s16x4 lds_tr(LAS unsigned char* L, u32 off) { return __builtin_amdgcn_ds_read_tr16_b64_v4i16((LAS s16x4*)(L + off)); }
__device__ __forceinline__ s16x4 lds_b64(LAS unsigned char* L, u32 off) { return *(LAS s16x4*)(L + off); }
__device__ __forceinline__ u32 lds_get(LAS unsigned char* L, u32 off) { return __hip_atomic_load((LAS u32*)(L + off), __ATOMIC_RELAXED, __HIP_MEMORY_SCOPE_WORKGROUP); }
__device__ __forceinline__ void lds_set(LAS unsigned char* L, u32 off, u32 v) { __hip_atomic_store((LAS u32*)(L + off), v, __ATOMIC_RELAXED, __HIP_MEMORY_SCOPE_WORKGROUP); }
__device__ __forceinline__ void lds_inc(LAS unsigned char* L, u32 off) { __hip_atomic_fetch_add((LAS u32*)(L + off), 1u, __ATOMIC_RELAXED, __HIP_MEMORY_SCOPE_WORKGROUP); }
__device__ __forceinline__ void lds_wait4(LAS unsigned char* L, u32 off) {
#pragma unroll 1
  for (u32 spin = 0; spin < LDS_CAP; ++spin) {
    if (__all(lds_get(L, off + (__lane_id() & 3) * 4) != 0u)) break;
    __builtin_amdgcn_s_sleep(1);
  }
  asm volatile("" ::: "memory");
}
template <bool SLEEP> __device__ __forceinline__ void lds_wait16(LAS unsigned char* L, u32 off) {
#pragma unroll 1
  for (u32 spin = 0; spin < LDS_CAP; ++spin) {
    if (lds_get(L, off) == 16u) break;
    if (SLEEP) __builtin_amdgcn_s_sleep(1);
  }
  asm volatile("" ::: "memory");
}
__device__ __forceinline__ void wait_hint(__amdgpu_buffer_rsrc_t r, u32 off, u32 tag) {
#pragma unroll 1
  for (u32 spin = 0; spin < SPIN_CAP; ++spin) {
    asm volatile("" ::: "memory");
    if (__all(__builtin_amdgcn_raw_buffer_load_b32(r, off, 0, AUX_SC1) == tag)) break;
  }
}

__global__ void __launch_bounds__(NTHR, 4) fused_kernel(Params p) {
  extern __shared__ __attribute__((aligned(16))) unsigned char smem[];
  LAS unsigned char* L = (LAS unsigned char*)smem;
  const u32 lb = (u32)(uintptr_t)L;
  const int tid = threadIdx.x, lane = tid & 63, w = __builtin_amdgcn_readfirstlane(tid >> 6);
  const int wg = blockIdx.x;
  const int idx = wg >> 3, hkv = (wg & 7) * 2 + (idx >> 4), ii = idx & 15, b = ii >> 3, s = ii & 7;
  const int hc = (hkv + 2) & 15, rb = ii >> 1, cg = ii & 1, u = hc * 8 + rb;
  const int fb = wg / 72, c72 = wg - 72 * fb, fcg = c72 / 36, ch = c72 - 36 * fcg;
  const __amdgpu_buffer_rsrc_t rws = __builtin_amdgcn_make_buffer_rsrc((void*)p.ws, 0, WS_END, 0x00020000);
  const int g4 = lane >> 4, col = lane & 15, q4 = col >> 2, p4 = lane & 3;

  if (w < 4) {
    const int cb = ii & 3, kq = ii >> 2;
    const u32 voff = (lane >> 3) * 8192 + (lane & 7) * 16;
    const char* base = (const char*)p.wq + (size_t)(144 * kq + 8 * w) * 8192 + (hkv * 256 + 64 * cb) * 2;
    if (w < 2) glds16<false>(p.x, w * 1024 + lane * 16, lb + L_X + w * 1024);
    else if (w == 2) { if (lane < 16) glds16<false>(p.x, 2048 + lane * 16, lb + L_X + 2048); }
#pragma unroll 1
    for (int j = w; j < 18; j += 4) { glds16<true>(base, voff, lb + L_WQ + j * 1024); base += 32 * 8192; }
  } else if (w == 15) {
    if (lane < 32) lds_set(L, L_FLAG + lane * 4, 0u);
    asm volatile("s_waitcnt lgkmcnt(0)" ::: "memory");
  }
  __builtin_amdgcn_s_barrier();
  u32 epoch;
  asm volatile("s_load_dword %0, %1, 0x0" : "=s"(epoch) : "s"(p.ws) : "memory");

  if (w >= 4 && w < 8 && wg < 216) {
    const int cb = wg / 3, kt = wg - 3 * cb;
    const u32 voff = (lane >> 3) * 9216 + (lane & 7) * 16;
    const char* base = (const char*)p.w1 + (size_t)(192 * kt + 8 * (w - 4)) * 9216 + cb * 128;
#pragma unroll 1
    for (int j = w - 4; j < 24; j += 4) { glds16<true>(base, voff, lb + L_W1 + j * 1024); base += 32 * 9216; }
  }
  if (w == 14) {
    u32 o = WS_HP + 36 * u * 8;
    if (lane < 36) o = WS_P2 + (((cg * 2 + (lane >= 18)) * 36 + 2 * (lane >= 18 ? lane - 18 : lane)) * 128 + u) * 64;
    else if (lane < 44) o = WS_P2 + (((fcg * 2 + (fb & 1)) * 36 + ch) * 128) * 64 + (lane - 36) * 1024;
    else if (lane == 44) o = WS_F1 + (hc * 16 + ii) * 128;
    else if (lane == 45) o = WS_F2 + (fcg * NREP + wg % NREP) * 128;
    else if (lane == 46) o = WS_P1 + ((hc * 8 + rb) * 16) * 288;
    else if (lane == 47) o = WS_P1 + ((hkv * 8) * 16 + b * 8 + s) * 288;
    else if (lane == 48) o = WS_P1 + ((hkv * 8 + 7) * 16 + b * 8 + s) * 288;
    else if (lane == 49) o = WS_F1 + hkv * 16 * 128;
    else if (lane == 50) o = WS_F2 + cg * NREP * 128;
    const unsigned char* a = p.ws + o;
    if (lane >= 52 && lane < 56) a = (const unsigned char*)p.wo + (size_t)(hc * 256 + 32 * rb + (lane - 52) * 10) * 1152 + cg * 576;
    else if (lane >= 56 && lane < 60) a = (const unsigned char*)p.w2 + (size_t)(36 * u + (lane - 56) * 11) * 1152 + cg * 576;
    else if (lane == 60) a = (const unsigned char*)(p.b1 + 36 * u);
    else if (lane == 61) a = (const unsigned char*)(p.b2 + fcg * 288 + ch * 8);
    else if (lane == 62 && wg < 144) a = (const unsigned char*)(p.out + fb * D + fcg * 288 + ch * 8);
    unsigned keep;
    asm volatile("s_mov_b32 %0, m0\n\ts_mov_b32 m0, %2\n\ts_nop 0\n\tglobal_load_lds_dword %1, off sc1\n\ts_mov_b32 m0, %0" : "=&s"(keep) : "v"(a), "s"(lb + L_JUNK) : "memory");
  }
  u32x4 ggw[2];
  if (w == 8) {
    const GAS u32x4* gp = (const GAS u32x4*)p.gw;
    ggw[0] = gp[lane]; ggw[1] = gp[64 + (lane & 7)];
  }
  const __amdgpu_buffer_rsrc_t rk = __builtin_amdgcn_make_buffer_rsrc((void*)p.K, 0, 0x10000000, 0x00020000);
  const __amdgpu_buffer_rsrc_t rv = __builtin_amdgcn_make_buffer_rsrc((void*)p.V, 0, 0x10000000, 0x00020000);
  const u32 kvoff = (u32)(((u32)(b * HKV + hkv) * SK + (SK - WIN) + s * 512 + w * 32) * (DH * 2));

  u64 tpub = 0;
  if (w < 4) {
    asm volatile("s_waitcnt vmcnt(0) lgkmcnt(0)" : "+s"(epoch) :: "memory");
    if (lane == 0) lds_set(L, FL_WQ + w * 4, 1u);
    lds_wait4(L, FL_WQ);
    const int cb = ii & 3, kq = ii >> 2;
    const u32 aoff = L_X + (lane & 1) * 1152 + (144 * kq + 4 * g4) * 2;
    const u32 boff = L_WQ + (4 * g4 + q4) * 128 + p4 * 8 + w * 32;
    s16x4 fa[9], fb_[9];
#pragma unroll
    for (int st = 0; st < 9; ++st) { fa[st] = lds_b64(L, aoff + st * 32); fb_[st] = lds_tr(L, boff + st * 2048); }
    f32x4 acc = {0.f, 0.f, 0.f, 0.f};
#pragma unroll
    for (int st = 0; st < 9; ++st) acc = mfma16(fa[st], fb_[st], acc);
    if (lane < 32) {
      const u32 o0 = WS_QP + (((hkv * 2 + g4) * 4 + kq) * 256 + 64 * cb + 16 * w + col) * 8;
      __builtin_amdgcn_raw_buffer_store_b64(u32x2{__float_as_uint(g4 ? acc[1] : acc[0]), epoch + 1u}, rws, o0, 0, AUX_SC1);
    }
    tpub = __builtin_amdgcn_s_memrealtime();
  }

  u32x4 kk[8], vv[8];
#pragma unroll
  for (int j = 0; j < 8; ++j) kk[j] = __builtin_amdgcn_raw_buffer_load_b128(rk, lane * 16, kvoff + j * 1024, AUX_NT);
#pragma unroll
  for (int j = 0; j < 8; ++j) vv[j] = __builtin_amdgcn_raw_buffer_load_b128(rv, lane * 16, kvoff + j * 1024, AUX_NT);
  __builtin_amdgcn_sched_barrier(0);
  if (lane == 0) lds_inc(L, C_ISSUED);
  asm volatile("s_waitcnt lgkmcnt(0)" : "+s"(epoch) :: "memory");
  const u32 tag = epoch + 1u;

  if (w < 4) {
    const u32 go = WS_QP + (((hkv * 2 + b) * 4 + (lane >> 5)) * 256 + 64 * w + 2 * (lane & 31)) * 8;
    while (__builtin_amdgcn_s_memrealtime() - tpub < QDELAY) __builtin_amdgcn_s_sleep(1);
    u32x4 qa, qb;
#pragma unroll 1
    for (u32 spin = 0;; ++spin) {
      asm volatile("" ::: "memory");
      qa = __builtin_amdgcn_raw_buffer_load_b128(rws, go, 0, AUX_SC1);
      qb = __builtin_amdgcn_raw_buffer_load_b128(rws, go + 2 * 2048, 0, AUX_SC1);
      if (__all((qa[1] == tag) & (qa[3] == tag) & (qb[1] == tag) & (qb[3] == tag)) || spin > SPIN_CAP) break;
      __builtin_amdgcn_s_sleep(8);
    }
    const float c0 = x32_sum(__uint_as_float(qa[0]) + __uint_as_float(qb[0])), c1 = x32_sum(__uint_as_float(qa[2]) + __uint_as_float(qb[2]));
    if (lane < 32) *(LAS u32*)(L + L_QS + w * 128 + lane * 4) = cvtpk(c0, c1);
    asm volatile("s_waitcnt lgkmcnt(0)" ::: "memory");
    if (lane == 0) lds_set(L, FL_Q + w * 4, 1u);
  } else if (w < 8) {
    asm volatile("s_waitcnt vmcnt(16)" ::: "memory");
    if (lane == 0) lds_set(L, FL_W1 + (w - 4) * 4, 1u);
    if (wg < 216) {
      lds_wait4(L, FL_WQ);
      lds_wait4(L, FL_W1);
      const int cb = wg / 3, kt = wg - 3 * cb;
      const u32 aoff = L_X + (lane & 1) * 1152 + (192 * kt + 4 * g4) * 2;
      const u32 boff = L_W1 + (4 * g4 + q4) * 128 + p4 * 8 + (w - 4) * 32;
      f32x4 acc = {0.f, 0.f, 0.f, 0.f};
#pragma unroll
      for (int hf = 0; hf < 2; ++hf) {
        s16x4 fa[6], fb_[6];
#pragma unroll
        for (int st = 0; st < 6; ++st) { fa[st] = lds_b64(L, aoff + (hf * 6 + st) * 32); fb_[st] = lds_tr(L, boff + (hf * 6 + st) * 2048); }
#pragma unroll
        for (int st = 0; st < 6; ++st) acc = mfma16(fa[st], fb_[st], acc);
      }
      if (lane < 32) {
        const u32 o0 = WS_HP + ((kt * 2 + g4) * F + 64 * cb + 16 * (w - 4) + col) * 8;
        __builtin_amdgcn_raw_buffer_store_b64(u32x2{__float_as_uint(g4 ? acc[1] : acc[0]), tag}, rws, o0, 0, AUX_SC1);
      }
    }
  } else if (w == 8) {
    lds_wait4(L, FL_WQ);
    float s0 = 0.f, s1 = 0.f;
#pragma unroll
    for (int r = 0; r < 2; ++r) {
      const u32 xo = L_X + (r ? 64 + (lane & 7) : lane) * 16;
      const u32x4 x0 = *(LAS u32x4*)(L + xo), x1 = *(LAS u32x4*)(L + xo + 1152);
      float t0 = 0.f, t1 = 0.f;
#pragma unroll
      for (int j = 0; j < 4; ++j) {
        const float glo = bf_lo(ggw[r][j]), ghi = bf_hi(ggw[r][j]);
        t0 += rbf(bf_lo(x0[j]) * glo) + rbf(bf_hi(x0[j]) * ghi);
        t1 += rbf(bf_lo(x1[j]) * glo) + rbf(bf_hi(x1[j]) * ghi);
      }
      if (r == 0 || lane < 8) { s0 += t0; s1 += t1; }
    }
    s0 += dpp<XOR1>(s0); s0 += dpp<XOR2>(s0); s0 += dpp<ROR4>(s0); s0 += dpp<ROR8>(s0); s0 = x32_sum(x16_sum(s0));
    s1 += dpp<XOR1>(s1); s1 += dpp<XOR2>(s1); s1 += dpp<ROR4>(s1); s1 += dpp<ROR8>(s1); s1 = x32_sum(x16_sum(s1));
    const float g0 = rbf(1.f / (1.f + __expf(-rbf(s0)))), g1 = rbf(1.f / (1.f + __expf(-rbf(s1))));
    if (lane == 0) *(LAS f32x4*)(L + L_G) = f32x4{g0, g1, rbf(1.f - g0), rbf(1.f - g1)};
  }

  lds_wait4(L, FL_Q);
  const u32x4 q0 = *(LAS u32x4*)(L + L_QS + col * 16), q1 = *(LAS u32x4*)(L + L_QS + 256 + col * 16);
  float x[16];
#pragma unroll
  for (int i = 0; i < 8; ++i) {
    float d0 = 0.f, d1 = 0.f;
#pragma unroll
    for (int j = 0; j < 4; ++j) { d0 = dot2(kk[i][j], q0[j], d0); d1 = dot2(kk[i][j], q1[j], d1); }
    x[2 * i] = d0; x[2 * i + 1] = d1;
  }
#define TR_STEP(HALF, CTRL, BIT) _Pragma("unroll") for (int h = 0; h < (HALF); ++h) { \
    const float lo_ = x[h], hi_ = x[h + (HALF)]; const float t1_ = lo_ + dpp<CTRL>(lo_), t2_ = hi_ + dpp<CTRL>(hi_); \
    x[h] = (col & (BIT)) ? t2_ : t1_; }
  TR_STEP(8, ROR8, 8) TR_STEP(4, XOR7, 4) TR_STEP(2, XOR2, 2) TR_STEP(1, XOR1, 1)
#undef TR_STEP
  const float sc = rbf(rbf(x[0]) * 0.08838834764831845f) * LOG2E;
  float mx = sc;
  mx = fmaxf(mx, dpp<XOR2>(mx)); mx = fmaxf(mx, dpp<ROR4>(mx)); mx = fmaxf(mx, dpp<ROR8>(mx)); mx = x32_max(x16_max(mx));
  const float pr = __builtin_amdgcn_exp2f(sc - mx);
  float ls = pr;
  ls += dpp<XOR2>(ls); ls += dpp<ROR4>(ls); ls += dpp<ROR8>(ls); ls = x32_sum(x16_sum(ls));
  if (lane < 2) { *(LAS float*)(L + L_MM + lane * 64 + w * 4) = mx; *(LAS float*)(L + L_ML + lane * 64 + w * 4) = ls; }
  f32x2 a0[4], a1[4];
#pragma unroll
  for (int j = 0; j < 4; ++j) { a0[j] = f32x2{0.f, 0.f}; a1[j] = f32x2{0.f, 0.f}; }
#define PV(i) { const float p0_ = dpp<BCAST + 2 * (i)>(pr), p1_ = dpp<BCAST + 2 * (i) + 1>(pr); \
    _Pragma("unroll") for (int j = 0; j < 4; ++j) { const f32x2 vf = {bf_lo(vv[i][j]), bf_hi(vv[i][j])}; \
      a0[j] = __builtin_elementwise_fma(vf, f32x2{p0_, p0_}, a0[j]); a1[j] = __builtin_elementwise_fma(vf, f32x2{p1_, p1_}, a1[j]); } }
  PV(0) PV(1) PV(2) PV(3) PV(4) PV(5) PV(6) PV(7)
#undef PV
  float y[8];
#pragma unroll
  for (int k = 0; k < 8; ++k) {
    auto r = __builtin_amdgcn_permlane32_swap(__float_as_uint(a0[k >> 1][k & 1]), __float_as_uint(a1[k >> 1][k & 1]), false, false);
    y[k] = __uint_as_float(r[0]) + __uint_as_float(r[1]);
  }
  f32x4 z;
#pragma unroll
  for (int k = 0; k < 4; ++k) {
    auto r = __builtin_amdgcn_permlane16_swap(__float_as_uint(y[k]), __float_as_uint(y[k + 4]), false, false);
    z[k] = __uint_as_float(r[0]) + __uint_as_float(r[1]);
  }
  *(LAS f32x4*)(L + L_MRG + w * 1024 + ((g4 >> 1) * 128 + 8 * col + 4 * (g4 & 1)) * 4) = z;
  asm volatile("s_waitcnt lgkmcnt(0)" ::: "memory");
  if (lane == 0) lds_inc(L, C_DONE);

  if (w == 5) {
    const int cc = lane < 36 ? lane : 35;
    const u32 ho = WS_HP + (36 * u + cc) * 8;
    u32x2 hg[6];
    const float bb = bf_lo(p.b1[36 * u + cc]);
#pragma unroll 1
    for (u32 spin = 0;; ++spin) {
      asm volatile("" ::: "memory");
#pragma unroll
      for (int j = 0; j < 6; ++j) hg[j] = __builtin_amdgcn_raw_buffer_load_b64(rws, ho + j * F * 8, 0, AUX_SC1);
      bool ok = true;
#pragma unroll
      for (int j = 0; j < 6; ++j) ok &= hg[j][1] == tag;
      if (__all(ok) || spin > SPIN_CAP) break;
      __builtin_amdgcn_s_sleep(16);
    }
#pragma unroll
    for (int bt = 0; bt < 2; ++bt) {
      float hs = __uint_as_float(hg[bt][0]) + __uint_as_float(hg[2 + bt][0]) + __uint_as_float(hg[4 + bt][0]);
      hs = rbf(rbf(hs) + bb);
      hs = hs > 0.f ? hs : 0.f;
      const u32 hv = lane < 36 ? (cvtpk(hs * hs, 0.f) & 0xffffu) : 0u;
      if (lane < 48) *(LAS u16*)(L + L_AH + bt * 96 + lane * 2) = (u16)hv;
    }
  }
  u32 bias2 = 0;
  if (w == 0 && wg < 144 && lane < 4) bias2 = *(const GAS u32*)(p.b2 + fcg * 288 + ch * 8 + 2 * lane);

  if (w >= 5 && w < 9) {
    lds_wait16<true>(L, C_ISSUED);
    const char* bo = (const char*)p.wo + (size_t)(hc * 256 + 32 * rb) * 1152 + cg * 576;
#pragma unroll 1
    for (int j = w - 5; j < 18; j += 4) { const u32 c = 64 * j + lane, row = (c * 1821u) >> 16; glds16<true>(bo, row * 576 + c * 16, lb + L_WO + j * 1024); }
    const char* b2 = (const char*)p.w2 + (size_t)(36 * u) * 1152 + cg * 576;
#pragma unroll 1
    for (int j = w - 5; j < 21; j += 4) { const u32 c = 64 * j + lane, row = (c * 1821u) >> 16; if (c < 1296) glds16<true>(b2, row * 576 + c * 16, lb + L_W2 + j * 1024); }
    asm volatile("s_waitcnt vmcnt(0) lgkmcnt(0)" ::: "memory");
    if (lane == 0) lds_set(L, FL_WD + (w - 5) * 4, 1u);
  }

  if (w < 4) {
    const int h = w >> 1, d = tid & 127;
    lds_wait16<false>(L, C_DONE);
    if (lane < 4) __builtin_amdgcn_raw_buffer_store_b32(tag, rws, WS_F1 + ((hkv * 16 + w * 4 + lane) * 32 + ii) * 4, 0, AUX_SC1);
    float ov[16];
#pragma unroll
    for (int j = 0; j < 16; ++j) ov[j] = *(LAS float*)(L + L_MRG + j * 1024 + tid * 4);
    const float mj = *(LAS float*)(L + L_MM + h * 64 + col * 4), lj = *(LAS float*)(L + L_ML + h * 64 + col * 4);
    float M = mj;
    M = fmaxf(M, dpp<XOR1>(M)); M = fmaxf(M, dpp<XOR2>(M)); M = fmaxf(M, dpp<ROR4>(M)); M = fmaxf(M, dpp<ROR8>(M));
    const float e = __builtin_amdgcn_exp2f(mj - M);
    float Ls = e * lj;
    Ls += dpp<XOR1>(Ls); Ls += dpp<XOR2>(Ls); Ls += dpp<ROR4>(Ls); Ls += dpp<ROR8>(Ls);
    float o = 0.f;
#pragma unroll
    for (int j = 0; j < 16; ++j) o = fmaf(__uint_as_float(__builtin_amdgcn_readlane(__float_as_uint(e), j)), ov[j], o);
    const u32 rec = WS_P1 + (((hkv * 8 + h * 4 + (d >> 5)) * 16) + b * 8 + s) * 288;
    __builtin_amdgcn_raw_buffer_store_b64(u32x2{__float_as_uint(o), tag}, rws, rec + (d & 31) * 8, 0, AUX_SC1);
    if ((d & 31) < 2) __builtin_amdgcn_raw_buffer_store_b64(u32x2{__float_as_uint((d & 1) ? Ls : M), tag}, rws, rec + 256 + (d & 1) * 8, 0, AUX_SC1);
  } else if (w == 4) {
    const int gb = lane >> 5, gs = (lane >> 2) & 7, dq = lane & 3;
    const u32 rec = WS_P1 + (((hc * 8 + rb) * 16) + gb * 8 + gs) * 288;
    lds_wait16<true>(L, C_DONE);
    wait_hint(rws, WS_F1 + ((hc * 16 + ii) * 32 + (lane & 15)) * 4, tag);
    if (rb == 0 && lane < NREP) __builtin_amdgcn_raw_buffer_store_b32(tag, rws, WS_F2 + ((cg * NREP + lane) * 32 + hc) * 4, 0, AUX_SC1);
    u32x4 og[4], ml;
#pragma unroll 1
    for (u32 spin = 0;; ++spin) {
      asm volatile("" ::: "memory");
#pragma unroll
      for (int j = 0; j < 4; ++j) og[j] = __builtin_amdgcn_raw_buffer_load_b128(rws, rec + dq * 64 + j * 16, 0, AUX_SC1);
      ml = __builtin_amdgcn_raw_buffer_load_b128(rws, rec + 256, 0, AUX_SC1);
      bool ok = (ml[1] == tag) & (ml[3] == tag);
#pragma unroll
      for (int j = 0; j < 4; ++j) ok &= (og[j][1] == tag) & (og[j][3] == tag);
      if (__all(ok) || spin > SPIN_CAP) break;
      __builtin_amdgcn_s_sleep(4);
    }
    const float m = __uint_as_float(ml[0]);
    float M = m;
    M = fmaxf(M, dpp<ROR4>(M)); M = fmaxf(M, dpp<ROR8>(M)); M = x16_max(M);
    const float e = __builtin_amdgcn_exp2f(m - M);
    float Ls = e * __uint_as_float(ml[2]);
    Ls += dpp<ROR4>(Ls); Ls += dpp<ROR8>(Ls); Ls = x16_sum(Ls);
    const float inv = 1.f / Ls;
    float ov[8];
#pragma unroll
    for (int k = 0; k < 8; ++k) {
      float t = e * __uint_as_float(og[k >> 1][(k & 1) * 2]);
      t += dpp<ROR4>(t); t += dpp<ROR8>(t); t = x16_sum(t);
      ov[k] = t * inv;
    }
    if (gs == 0) *(LAS u32x4*)(L + L_AO + gb * 64 + dq * 16) = u32x4{cvtpk(ov[0], ov[1]), cvtpk(ov[2], ov[3]), cvtpk(ov[4], ov[5]), cvtpk(ov[6], ov[7])};
  }

  if (w < 10 && w != 4) {
    const int mw = w < 4 ? w : w - 1;
    const u32 ao = L_AO + (lane & 1) * 64 + 4 * g4 * 2, ah = L_AH + (lane & 1) * 96 + 4 * g4 * 2;
    const int rr = 4 * g4 + q4, tb = g4 & 1, tt = g4 >> 1;
    lds_wait4(L, FL_WD);
    f32x4 df[2], da[2];
    s16x4 bo[2][2];
#pragma unroll
    for (int t = 0; t < 2; ++t) { df[t] = f32x4{0.f, 0.f, 0.f, 0.f}; da[t] = f32x4{0.f, 0.f, 0.f, 0.f}; }
#pragma unroll
    for (int st = 0; st < 3; ++st) {
      const s16x4 a = lds_b64(L, ah + st * 32);
      const int row = min(16 * st + rr, 35);
#pragma unroll
      for (int t = 0; t < 2; ++t) df[t] = mfma16(a, lds_tr(L, L_W2 + row * 576 + (2 * mw + t) * 32 + p4 * 8), df[t]);
    }
#pragma unroll
    for (int st = 0; st < 2; ++st)
#pragma unroll
      for (int t = 0; t < 2; ++t) bo[st][t] = lds_tr(L, L_WO + (16 * st + rr) * 576 + (2 * mw + t) * 32 + p4 * 8);
    const f32x4 gt = *(const LAS f32x4*)(L + L_G);
    const float ga = tb ? gt[1] : gt[0];
    const float vf = (tb ? gt[3] : gt[2]) * (tt ? (tb ? df[1][1] : df[1][0]) : (tb ? df[0][1] : df[0][0]));
    const int n = (2 * mw + tt) * 16 + col;
    const u32 off = WS_P2 + ((((cg * 2 + tb) * 36 + (n >> 3)) * 128) + u) * 64 + (n & 7) * 8;
    asm volatile("" :: "v"(vf), "v"(off));
    asm volatile("s_waitcnt lgkmcnt(0)\n\ts_barrier" ::: "memory");
#pragma unroll
    for (int st = 0; st < 2; ++st) {
      const s16x4 a = lds_b64(L, ao + st * 32);
#pragma unroll
      for (int t = 0; t < 2; ++t) da[t] = mfma16(a, bo[st][t], da[t]);
    }
    const float val = ga * (tt ? (tb ? da[1][1] : da[1][0]) : (tb ? da[0][1] : da[0][0])) + vf;
    __builtin_amdgcn_raw_buffer_store_b64(u32x2{__float_as_uint(val), tag}, rws, off, 0, AUX_SC1);
  } else {
    asm volatile("s_waitcnt lgkmcnt(0)\n\ts_barrier" ::: "memory");
  }

  if (wg >= 144 || w != 0) return;
  {
    const u32 off = WS_P2 + (((fcg * 2 + fb) * 36 + ch) * 128) * 64 + lane * 16;
    wait_hint(rws, WS_F2 + ((fcg * NREP + wg % NREP) * 32 + (lane & 15)) * 4, tag);
    u32x4 pg[8];
#pragma unroll 1
    for (u32 spin = 0;; ++spin) {
      asm volatile("" ::: "memory");
#pragma unroll
      for (int j = 0; j < 8; ++j) pg[j] = __builtin_amdgcn_raw_buffer_load_b128(rws, off + j * 1024, 0, AUX_SC1);
      bool ok = true;
#pragma unroll
      for (int j = 0; j < 8; ++j) ok &= (pg[j][1] == tag) & (pg[j][3] == tag);
      if (__all(ok) || spin > SPIN_CAP) break;
      __builtin_amdgcn_s_sleep(4);
    }
    float t0 = 0.f, t1 = 0.f;
#pragma unroll
    for (int j = 0; j < 8; ++j) { t0 += __uint_as_float(pg[j][0]); t1 += __uint_as_float(pg[j][2]); }
    t0 += dpp<ROR4>(t0); t0 += dpp<ROR8>(t0); t0 = x32_sum(x16_sum(t0));
    t1 += dpp<ROR4>(t1); t1 += dpp<ROR8>(t1); t1 = x32_sum(x16_sum(t1));
    const float omg = ((const LAS float*)(L + L_G))[2 + fb];
    t0 += omg * bf_lo(bias2); t1 += omg * bf_hi(bias2);
    if (lane < 4) *(GAS u32*)(p.out + fb * D + fcg * 288 + ch * 8 + 2 * lane) = cvtpk(t0, t1);
    if (wg == 0 && lane == 0) __hip_atomic_store((GAS u32*)(p.ws + WS_E), tag, __ATOMIC_RELAXED, __HIP_MEMORY_SCOPE_AGENT);
  }
}

extern "C" void kernel_launch(void* const* d_in, const int* in_sizes, int n_in, void* d_out, int out_size, void* d_ws, size_t ws_size,
                              hipStream_t stream) {
  Params p;
  p.x = (const u16*)d_in[0]; p.K = (const u16*)d_in[1]; p.V = (const u16*)d_in[2]; p.wq = (const u16*)d_in[3]; p.wo = (const u16*)d_in[4];
  p.w1 = (const u16*)d_in[6]; p.b1 = (const u16*)d_in[7]; p.w2 = (const u16*)d_in[8]; p.b2 = (const u16*)d_in[9]; p.gw = (const u16*)d_in[10];
  p.out = (u16*)d_out; p.ws = (unsigned char*)d_ws;
  (void)hipFuncSetAttribute((const void*)fused_kernel, hipFuncAttributeMaxDynamicSharedMemorySize, L_END);
  hipLaunchKernelGGL(fused_kernel, dim3(NWG), dim3(NTHR), L_END, stream, p);
}
```
